```python
import torch

def _softmax(x, axis=-1):
    return torch.softmax(x, dim=axis)
def _gelu(x):
    return torch.nn.functional.gelu(x, approximate="tanh")
def _silu(x):
    return torch.nn.functional.silu(x)
def _sigmoid(x):
    return torch.sigmoid(x)
def _layernorm(x, axis=-1, eps=1e-5):
    m = x.mean(dim=axis, keepdim=True)
    v = x.var(dim=axis, keepdim=True, unbiased=False)
    return (x - m) / torch.sqrt(v + eps)
def _scatter_add(tgt, idx, src, axis=0):
    return tgt.clone().index_add_(axis, idx, src)
def _segment_sum(src, seg, n):
    out = torch.zeros((n,) + src.shape[1:], dtype=src.dtype)
    return out.index_add_(0, seg, src)
def _pad(x, pw):
    flat = tuple(v for pair in reversed(pw) for v in pair)
    return torch.nn.functional.pad(x, flat)

kernel_name = 'gated_mix__spectral_fnet__glu_gated_glu__prompt'

def setup_inputs():

    _w = (lambda a: (lambda t: t.to(torch.bfloat16) if t.is_floating_point() else t)(torch.as_tensor(a)))
    def _A_setup():
        B, S, D = 1, 2048, 640
        ns = torch.arange(S)[:, None] * torch.arange(S)[None, :]
        nd = torch.arange(D)[:, None] * torch.arange(D)[None, :]
        Ws_re = torch.cos(-2.0 * torch.pi * ns / S).to(float)
        Ws_im = torch.sin(-2.0 * torch.pi * ns / S).to(float)
        Wd_re = torch.cos(-2.0 * torch.pi * nd / D).to(float)
        Wd_im = torch.sin(-2.0 * torch.pi * nd / D).to(float)
        return {
            "x": _w((lambda *s: torch.randn(*s, dtype=torch.bfloat16) * 0.05)(B, S, D)),
            "dft_seq_re": _w(Ws_re),
            "dft_seq_im": _w(Ws_im),
            "dft_hid_re": _w(Wd_re),
            "dft_hid_im": _w(Wd_im),
        }
    _A_in = _A_setup()

    def _B_setup():
        B, S, D, F = 1, 2048, 640, 1536
        return {
            "x": _w((lambda *s: torch.randn(*s, dtype=torch.bfloat16) * 0.05)(B, S, D)),
            "w_gate": _w((lambda *s: torch.randn(*s, dtype=torch.bfloat16) * 1.6)(D, F)),
            "w_up": _w((lambda *s: torch.randn(*s, dtype=torch.bfloat16) * 0.8)(D, F)),
            "w_down": _w((lambda *s: torch.randn(*s, dtype=torch.bfloat16) * 0.8)(F, D)),
        }
    _B_in = _B_setup()

    return {
        "A_x": _A_in["x"],
        "A_dft_seq_re": _A_in["dft_seq_re"],
        "A_dft_seq_im": _A_in["dft_seq_im"],
        "A_dft_hid_re": _A_in["dft_hid_re"],
        "A_dft_hid_im": _A_in["dft_hid_im"],
        "B_x": _B_in["x"],
        "B_w_gate": _B_in["w_gate"],
        "B_w_up": _B_in["w_up"],
        "B_w_down": _B_in["w_down"],
        "gate_w": _w((lambda *s: torch.randn(*s, dtype=torch.bfloat16) * 0.05)(640) * 16.0),
    }

def reference(A_x, A_dft_seq_re, A_dft_seq_im, A_dft_hid_re, A_dft_hid_im, B_x, B_w_gate, B_w_up, B_w_down, gate_w):
    def _A_ref(x, dft_seq_re, dft_seq_im, dft_hid_re, dft_hid_im):
        a = torch.einsum("bsd,ed->bse", x, dft_hid_re)
        b = torch.einsum("bsd,ed->bse", x, dft_hid_im)
        y = torch.einsum("ts,bse->bte", dft_seq_re, a) - torch.einsum("ts,bse->bte", dft_seq_im, b)
        return y

    def _B_ref(x, w_gate, w_up, w_down):
        g = torch.einsum("bsd,df->bsf", x, w_gate)
        u = torch.einsum("bsd,df->bsf", x, w_up)
        h = _sigmoid(g) * u
        return torch.einsum("bsf,fd->bsd", h, w_down)

    x = A_x
    g = _sigmoid((x * gate_w).sum(-1, keepdims=True))
    a = _A_ref(x, A_dft_seq_re, A_dft_seq_im, A_dft_hid_re, A_dft_hid_im)
    b = _B_ref(x, B_w_gate, B_w_up, B_w_down)
    return g * a + (1 - g) * b
```

```cpp
#include <hip/hip_runtime.h>
#include <hip/hip_bf16.h>
#include <stdint.h>

typedef __attribute__((ext_vector_type(8))) __bf16 bf16x8;
typedef __attribute__((ext_vector_type(2))) __bf16 bf16x2;
typedef __attribute__((ext_vector_type(4))) short s16x4;
typedef __attribute__((ext_vector_type(8))) short s16x8;
typedef __attribute__((ext_vector_type(4))) float f32x4;
typedef __attribute__((ext_vector_type(2))) float f32x2;
typedef __attribute__((ext_vector_type(4))) unsigned u32x4;
typedef __attribute__((ext_vector_type(2))) unsigned u32x2;
typedef __attribute__((address_space(3))) char* lds_p;
typedef __attribute__((address_space(1))) unsigned gu32;
typedef __attribute__((address_space(3))) unsigned lds_u32;
typedef __amdgpu_buffer_rsrc_t rsrc_t;

#define DEV __device__ __forceinline__

constexpr int SEQ = 2048, DM = 640, FF = 1536;
constexpr int N3A = 17;
constexpr int SLOT_A = 49152, SLOT_B = 36864;
constexpr int SPARE = 147456;
constexpr int Z_OFF = SPARE + 15360;
constexpr int RDY_OFF = SPARE + 15872;
constexpr int PK_EP = SPARE + 15876, PK_AB = SPARE + 15888, PK_SL = PK_AB + 64, PK_H = SPARE + 16128;
constexpr int LDS_BYTES = 163840;
constexpr unsigned WS_FLAG = 0;
constexpr unsigned FL_AB = 128, FL_H = 2048, FL_WD = 4096, FL_SL = 8192;
constexpr unsigned WS_AB = 16384;
constexpr unsigned WS_H = WS_AB + 2 * 4 * 32 * 20480;
constexpr unsigned WS_WD = WS_H + 16 * 2 * 12 * 16384;
constexpr unsigned WS_SL = WS_WD + 8 * 12 * 20480;
constexpr unsigned WS_END = WS_SL + 256 * 30720;
constexpr unsigned SPIN_MAX = 200000;

template <int N> DEV void waitv() { asm volatile("s_waitcnt vmcnt(%0)" ::"n"(N) : "memory"); }
DEV void bar() {
  asm volatile("" ::: "memory");
  __builtin_amdgcn_s_barrier();
  asm volatile("" ::: "memory");
}
DEV void wait_lds() { __builtin_amdgcn_s_waitcnt(0xc07f); }
#define SCHED0 __builtin_amdgcn_sched_barrier(0)

DEV void dma1(rsrc_t r, unsigned voff, unsigned soff, unsigned ldsb) {
  asm volatile("s_mov_b32 m0, %0\n\ts_nop 3\n\tbuffer_load_dwordx4 %1, %2, %3 offen lds" ::"s"(ldsb), "v"(voff), "s"(r), "s"(soff) : "memory");
}
#define DMA4_BODY(POL) "s_mov_b32 m0, %0\n\ts_nop 0\n\t" \
  "buffer_load_dwordx4 %1, %5, %6 offen" POL " lds\n\tbuffer_load_dwordx4 %2, %5, %6 offen offset:1024" POL " lds\n\t" \
  "buffer_load_dwordx4 %3, %5, %6 offen offset:2048" POL " lds\n\tbuffer_load_dwordx4 %4, %5, %6 offen offset:3072" POL " lds"
template <int AUX> DEV void dma4x(rsrc_t r, const unsigned (&v)[4], unsigned soff, unsigned ldsb) {
  if constexpr (AUX == 0) asm volatile(DMA4_BODY("") ::"s"(ldsb), "v"(v[0]), "v"(v[1]), "v"(v[2]), "v"(v[3]), "s"(r), "s"(soff) : "memory");
  else if constexpr (AUX == 16) asm volatile(DMA4_BODY(" sc1") ::"s"(ldsb), "v"(v[0]), "v"(v[1]), "v"(v[2]), "v"(v[3]), "s"(r), "s"(soff) : "memory");
  else asm volatile(DMA4_BODY(" nt") ::"s"(ldsb), "v"(v[0]), "v"(v[1]), "v"(v[2]), "v"(v[3]), "s"(r), "s"(soff) : "memory");
}
DEV void dma2s(rsrc_t r, unsigned v, unsigned soff, unsigned ldsb) {
  asm volatile("s_mov_b32 m0, %0\n\ts_nop 0\n\tbuffer_load_dwordx4 %1, %2, %3 offen sc1 lds\n\tbuffer_load_dwordx4 %1, %2, %3 offen offset:1024 sc1 lds"
               ::"s"(ldsb), "v"(v), "s"(r), "s"(soff) : "memory");
}
DEV rsrc_t mk_rsrc(const void* p, unsigned bytes) { return __builtin_amdgcn_make_buffer_rsrc((void*)p, (short)0, (int)bytes, 0x00020000); }
DEV bf16x8 ldf(lds_p p) { return *(const __attribute__((address_space(3))) bf16x8*)p; }
DEV bf16x8 ldtr(lds_p p) {
  s16x4 lo = __builtin_amdgcn_ds_read_tr16_b64_v4i16((__attribute__((address_space(3))) s16x4*)p);
  s16x4 hi = __builtin_amdgcn_ds_read_tr16_b64_v4i16((__attribute__((address_space(3))) s16x4*)(p + 512));
  s16x8 v = __builtin_shufflevector(lo, hi, 0, 1, 2, 3, 4, 5, 6, 7);
  return __builtin_bit_cast(bf16x8, v);
}
DEV f32x4 mma(bf16x8 a, bf16x8 b, f32x4 c) { return __builtin_amdgcn_mfma_f32_16x16x32_bf16(a, b, c, 0, 0, 0); }
DEV unsigned pk2(float a, float b) { f32x2 v = {a, b}; return __builtin_bit_cast(unsigned, __builtin_convertvector(v, bf16x2)); }
DEV u32x4 pk8(f32x4 a, f32x4 b) { u32x4 r = {pk2(a[0], a[1]), pk2(a[2], a[3]), pk2(b[0], b[1]), pk2(b[2], b[3])}; return r; }
DEV float bflo(unsigned v) { return __builtin_bit_cast(float, v << 16); }
DEV float bfhi(unsigned v) { return __builtin_bit_cast(float, v & 0xffff0000u); }
DEV void st16(u32x4 v, rsrc_t r, unsigned off) { __builtin_amdgcn_raw_buffer_store_b128(v, r, off, 0, 16); }
DEV void st8(u32x2 v, rsrc_t r, unsigned off) { __builtin_amdgcn_raw_buffer_store_b64(v, r, off, 0, 16); }
template <int NM, int NR> DEV void interleave() {
#pragma unroll
  for (int i = 0; i < NR; ++i) {
    __builtin_amdgcn_sched_group_barrier(0x008, 1, 0);
    __builtin_amdgcn_sched_group_barrier(0x100, 1, 0);
  }
  if constexpr (NM > NR) __builtin_amdgcn_sched_group_barrier(0x008, NM - NR, 0);
}
#define STAMP(i) do {} while (0)

DEV f32x4 glu4(f32x4 G, f32x4 U, float c) {
  f32x4 r;
#pragma unroll
  for (int j = 0; j < 4; ++j) {
    float e = __builtin_amdgcn_exp2f(G[j] * -1.44269504088896f);
    r[j] = U[j] * __builtin_amdgcn_rcpf(__builtin_fmaf(e, c, c));
  }
  return r;
}

DEV void wait_rdy(lds_p sm, int lvl) {
  for (unsigned spin = 0; spin < SPIN_MAX; ++spin) {
    if (__builtin_amdgcn_readfirstlane(*(volatile lds_u32*)(sm + RDY_OFF)) >= lvl) break;
    __builtin_amdgcn_s_sleep(1);
  }
}
#define RND(P, I, W) do { I; W; bar(); } while (0)
DEV unsigned nexts(unsigned sb) { return sb == 3 * SLOT_B ? 0u : sb + SLOT_B; }

template <int LW> DEV void loader(lds_p sm, const char* x, const char* wsq, const char* wd, const char* wg, const char* wu, rsrc_t rws,
                                  int lane, int rt, int ct, int kq, char* wsb) {
  const int odd = kq & 1, mm = kq >> 1;
  const int lr = lane >> 3, lp = lane & 7;
  const unsigned chT = (lp ^ (lr & 6)) * 16;
  unsigned vx[4], vp[4], vs[4], vw[4], vl[4];
#pragma unroll
  for (int j = 0; j < 4; ++j) {
    vx[j] = (lr + 8 * j) * (DM * 2) + chT - j * 1024;
    vp[j] = (8 * (lane >> 5) + (lr & 3) + 16 * (j & 1) + 4 * (j >> 1)) * (DM * 2) + chT - j * 1024;
    vs[j] = (lr + 8 * j) * (SEQ * 2) + chT - j * 1024;
    vw[j] = (lr + 8 * j) * (FF * 2) + (((lp >> 1) ^ (((lr >> 1) & 1) | ((j & 1) << 1))) * 2 + (lp & 1)) * 16 - j * 1024;
    vl[j] = lane * 16;
  }
  const rsrc_t rx = mk_rsrc(x, SEQ * DM * 2);
  const rsrc_t rw = mk_rsrc(LW == 2 ? wu : wg, DM * FF * 2);
  unsigned sb = 0;
  if (!odd) {
    unsigned so = LW == 0 ? rt * 128 * (DM * 2) : (4 * mm + ct) * 256;
    auto issue = [&]() {
      if constexpr (LW == 0) {
#pragma unroll
        for (int g = 0; g < 4; ++g) dma4x<0>(rx, vx, so + g * 32 * (DM * 2), sb + g * 4096);
        so += 128;
      } else {
#pragma unroll
        for (int g = 0; g < 4; ++g) dma4x<0>(rw, vw, so + (g & 1) * 32 * (FF * 2) + (g >> 1) * 128, sb + LW * 16384 + g * 4096);
        so += 64 * (FF * 2);
      }
      sb = sb == 2 * SLOT_A ? 0u : sb + SLOT_A;
    };
    issue(); issue(); issue();
    waitv<32>(); bar();
    waitv<16>(); bar();
#pragma unroll 1
    for (int r = 2; r <= 8; ++r) RND(0, issue(), waitv<16>());
    waitv<0>(); bar();
    bar();
    sb = (10 & 3) * SLOT_B;
  } else {
    const rsrc_t rwd = mk_rsrc(wd, DM * DM * 2);
    unsigned sx = rt * 128 * (DM * 2), sw = ct * 160 * (DM * 2);
    auto issue_a = [&]() {
      if constexpr (LW == 0) {
#pragma unroll
        for (int g = 0; g < 3; ++g) dma4x<0>(rx, vp, sx + g * 32 * (DM * 2), sb + g * 4096);
      } else if constexpr (LW == 1) {
        dma4x<0>(rx, vp, sx + 3 * 32 * (DM * 2), sb + 3 * 4096);
#pragma unroll
        for (int g = 0; g < 2; ++g) dma4x<0>(rwd, vx, sw + g * 32 * (DM * 2), sb + 16384 + g * 4096);
      } else {
#pragma unroll
        for (int g = 2; g < 5; ++g) dma4x<0>(rwd, vx, sw + g * 32 * (DM * 2), sb + 16384 + g * 4096);
      }
      sx += 128; sw += 128; sb = nexts(sb);
    };
    auto issue_b = [&]() {
      if constexpr (LW == 0) {
#pragma unroll
        for (int g = 0; g < 3; ++g) dma4x<0>(rx, vx, sx + g * 32 * (DM * 2), sb + g * 4096);
      } else {
        if constexpr (LW == 1) dma4x<0>(rx, vx, sx + 3 * 32 * (DM * 2), sb + 3 * 4096);
#pragma unroll
        for (int g = 0; g < 2; ++g) dma4x<0>(rw, vw, sw + g * 32 * (FF * 2), sb + 8192 + LW * 8192 + g * 4096);
      }
      sx += 128; sw += 64 * (FF * 2); sb = nexts(sb);
    };
    constexpr int PB = LW == 2 ? 8 : 12;
    issue_a(); issue_a(); issue_a(); issue_a();
    waitv<36>(); bar();
    waitv<24>(); bar();
#pragma unroll 1
    for (int r = 2; r <= 7; ++r) RND(0, issue_a(), waitv<24>());
    sx = rt * 128 * (DM * 2); sw = 2048 + (4 * mm + ct) * 128;
    issue_b(); waitv<12 + PB>(); bar();
    issue_b(); waitv<2 * PB>(); bar();
    issue_b(); bar();
    issue_b(); waitv<3 * PB>(); bar();
    waitv<2 * PB>(); bar();
#pragma unroll 1
    for (int r = 2; r <= 7; ++r) RND(1, issue_b(), waitv<2 * PB>());
  }
  const rsrc_t rsq = mk_rsrc(wsq, SEQ * SEQ * 2);
  const int n3 = odd ? 32 - N3A : N3A;
  unsigned sa = rt * 128 * (SEQ * 2) + (odd ? N3A : 0) * 128, sc = WS_AB + ((mm * 4 + ct) * 32 + (odd ? N3A : 0)) * 20480;
  auto issue_3 = [&]() {
    if constexpr (LW == 0) {
#pragma unroll
      for (int g = 0; g < 3; ++g) dma4x<0>(rsq, vs, sa + g * 32 * (SEQ * 2), sb + g * 4096);
    } else if constexpr (LW == 1) {
      dma4x<0>(rsq, vs, sa + 3 * 32 * (SEQ * 2), sb + 3 * 4096);
#pragma unroll
      for (int g = 0; g < 2; ++g) dma4x<16>(rws, vl, sc + g * 4096, sb + 16384 + g * 4096);
    } else {
#pragma unroll
      for (int g = 2; g < 5; ++g) dma4x<16>(rws, vl, sc + g * 4096, sb + 16384 + g * 4096);
    }
    sa += 128; sc += 20480; sb = nexts(sb);
  };
  auto issue_4 = [&]() {
    if constexpr (LW == 0) {
#pragma unroll
      for (int g = 0; g < 3; ++g) dma4x<16>(rws, vl, sa + g * 4096, sb + g * 4096);
    } else if constexpr (LW == 1) {
      dma4x<16>(rws, vl, sa + 3 * 4096, sb + 3 * 4096);
#pragma unroll
      for (int g = 0; g < 2; ++g) dma4x<16>(rws, vl, sc + g * 4096, sb + 16384 + g * 4096);
    } else {
#pragma unroll
      for (int g = 2; g < 5; ++g) dma4x<16>(rws, vl, sc + g * 4096, sb + 16384 + g * 4096);
    }
    sa += 16384; sc += 20480; sb = nexts(sb);
  };
  wait_rdy(sm, 1);
  if (!odd) {
    issue_3(); issue_3(); issue_3(); issue_3();
  } else {
    constexpr int PB = LW == 2 ? 8 : 12;
    issue_3(); waitv<PB + 12>(); bar();
    issue_3(); waitv<24>(); bar();
    issue_3(); bar();
    issue_3();
  }
  waitv<36>(); bar();
  waitv<24>(); bar();
#pragma unroll 1
  for (int r = 2; r <= n3 - 3; ++r) RND(2, issue_3(), waitv<24>());
  wait_rdy(sm, 2);
  sa = WS_H + (rt * 2 + mm) * 12 * 16384; sc = WS_WD + (ct * 2 + odd) * 12 * 20480;
  issue_4(); waitv<24>(); bar();
  issue_4(); waitv<24>(); bar();
  issue_4(); bar();
  issue_4(); waitv<36>(); bar();
  waitv<24>(); bar();
#pragma unroll 1
  for (int r = 2; r <= 9; ++r) RND(3, issue_4(), waitv<24>());
  const unsigned ss = WS_SL + ((rt * 4 + ct) * 4 + kq) * 30720 + LW * 10240;
  bool done = false;
#pragma unroll 1
  for (int r = 10; r <= 12; ++r) {
    if (!done) {
      if (r == 12) wait_rdy(sm, 3);
      if (r == 12 || __builtin_amdgcn_readfirstlane(*(volatile lds_u32*)(sm + RDY_OFF)) >= 3) {
        dma4x<16>(rws, vl, ss, sb + LW * 10240);
        dma4x<16>(rws, vl, ss + 4096, sb + LW * 10240 + 4096);
        dma2s(rws, vl[0], ss + 8192, sb + LW * 10240 + 8192);
        done = true;
      }
    }
    if (r == 10) { if (done) waitv<22>(); else waitv<12>(); }
    else if (r == 11 && done) waitv<10>();
    else waitv<0>();
    bar();
  }
}

__global__ __launch_bounds__(512, 2) void fused_kernel(const char* __restrict__ x, const char* __restrict__ ws_re, const char* __restrict__ ws_im,
                                                       const char* __restrict__ wd_re, const char* __restrict__ wd_im,
                                                       const char* __restrict__ wg, const char* __restrict__ wu, const char* __restrict__ wdn,
                                                       const unsigned* __restrict__ gate_w, char* __restrict__ out, char* wsb) {
  extern __shared__ __attribute__((aligned(16))) char smem_raw[];
  const lds_p sm = (lds_p)smem_raw;
  const int wid = __builtin_amdgcn_readfirstlane(threadIdx.x >> 6), lane = threadIdx.x & 63;
  const int bid = blockIdx.x, xcd = bid & 7, idx = bid >> 3;
  const int kq = xcd >> 1, rt = (xcd & 1) * 8 + (idx >> 2), ct = idx & 3;
  const int odd = kq & 1, mm = kq >> 1;
  const int b2 = odd ? 20 : 10, n3 = odd ? 32 - N3A : N3A, s30 = odd ? N3A : 0;
  const int b3 = b2 + n3, b4 = b3 + 12;
  const rsrc_t rws = mk_rsrc(wsb, WS_END);
  STAMP(0);

  if (wid < 4) {
    const int r16 = lane & 15, q = lane >> 4;
    const unsigned t0 = r16 * 128 + ((q ^ (r16 & 6)) << 4), t1 = t0 ^ 64;
    const int q4 = r16 >> 2, fx = ((q4 >> 1) & 1) | ((q & 1) << 1);
    const unsigned xrow = (8 * q + q4) * 128 + (lane & 3) * 8;
    const int hi = wid >> 1, lo = wid & 1;
    const unsigned l16 = lane * 16;
    if (!odd) {
      f32x4 acc[8][4] = {};
      bf16x8 xa[4], wa[8], xb[4], wb[8];
      unsigned xo[4];
#pragma unroll
      for (int m = 0; m < 4; ++m) xo[m] = xrow + ((m ^ fx) << 5) + 16384 + lo * 8192;
      const unsigned ta = t0 + hi * 8192, tb = t1 + hi * 8192;
#define RD_GUW(X, W, SB, TO, KS) do { _Pragma("unroll") for (int i = 0; i < 4; ++i) X[i] = ldf(sm + (SB) + (TO) + i * 2048); \
      _Pragma("unroll") for (int m = 0; m < 4; ++m) { W[m] = ldtr(sm + (SB) + xo[m] + (KS) * 4096); W[4 + m] = ldtr(sm + (SB) + xo[m] + 16384 + (KS) * 4096); } } while (0)
#define MM_GUW(X, W) do { _Pragma("unroll") for (int j = 0; j < 8; ++j) _Pragma("unroll") for (int i = 0; i < 4; ++i) acc[j][i] = mma(W[j], X[i], acc[j][i]); } while (0)
      bar();
      STAMP(1);
      RD_GUW(xa, wa, 0, ta, 0);
      unsigned sb = 0;
#pragma unroll 1
      for (int n = 0; n < 10; ++n) {
        SCHED0;
        RD_GUW(xb, wb, sb, tb, 1);
        MM_GUW(xa, wa);
        interleave<32, 20>();
        SCHED0;
        wait_lds(); bar();
        sb = (sb == 2 * SLOT_A) ? 0 : sb + SLOT_A;
        RD_GUW(xa, wa, sb, ta, 0);
        MM_GUW(xb, wb);
        interleave<32, 20>();
      }
      SCHED0;
      STAMP(2);
      const unsigned hbase = WS_H + ((rt * 2 + hi) * 12 + 4 * mm + ct) * 16384 + lo * 8192 + l16;
#pragma unroll
      for (int i = 0; i < 4; ++i) {
        float z = *(const __attribute__((address_space(3))) float*)(sm + Z_OFF + (hi * 64 + i * 16 + r16) * 4);
        float c = fminf(1.0f + __builtin_amdgcn_exp2f(z * 1.44269504088896f), 1e38f);
#pragma unroll
        for (int p = 0; p < 2; ++p)
          st16(pk8(glu4(acc[2 * p][i], acc[4 + 2 * p][i], c), glu4(acc[2 * p + 1][i], acc[5 + 2 * p][i], c)), rws, hbase + p * 4096 + i * 1024);
      }
    } else {
      {
        f32x4 acc[4][5] = {};
        bf16x8 xa[4], wa[5], xb[4], wb[5];
        const unsigned ta = t0 + hi * 8192, tb = t1 + hi * 8192, ua = t0 + 16384 + lo * 10240, ub = t1 + 16384 + lo * 10240;
#define RD_TT(X, W, SB, TO, UO) do { _Pragma("unroll") for (int i = 0; i < 4; ++i) X[i] = ldf(sm + (SB) + (TO) + i * 2048); \
        _Pragma("unroll") for (int j = 0; j < 5; ++j) W[j] = ldf(sm + (SB) + (UO) + j * 2048); } while (0)
#define MM_AB(X, W) do { _Pragma("unroll") for (int j = 0; j < 5; ++j) _Pragma("unroll") for (int i = 0; i < 4; ++i) acc[i][j] = mma(X[i], W[j], acc[i][j]); } while (0)
        bar();
        STAMP(1);
        RD_TT(xa, wa, 0, ta, ua);
#pragma unroll 1
        for (int n = 0; n < 10; ++n) {
          SCHED0;
          RD_TT(xb, wb, (n & 3) * SLOT_B, tb, ub);
          MM_AB(xa, wa);
          interleave<20, 9>();
          SCHED0;
          wait_lds(); bar();
          RD_TT(xa, wa, ((n + 1) & 3) * SLOT_B, ta, ua);
          MM_AB(xb, wb);
          interleave<20, 9>();
        }
        SCHED0;
        const unsigned abase = WS_AB + ((mm * 4 + ct) * 32 + rt * 2 + hi) * 20480 + lo * 10240 + l16;
        const unsigned sg = mm ? 0x80008000u : 0u;
#pragma unroll
        for (int j = 0; j < 5; ++j)
#pragma unroll
          for (int p = 0; p < 2; ++p) {
            u32x4 v = pk8(acc[2 * p][j], acc[2 * p + 1][j]);
            v ^= sg;
            st16(v, rws, abase + j * 2048 + p * 1024);
          }
      }
      STAMP(2);
      {
        f32x4 acc[4][4] = {};
        bf16x8 xa[4], wa[4], xb[4], wb[4];
        unsigned xo[2];
#pragma unroll
        for (int m = 0; m < 2; ++m) xo[m] = xrow + (((2 * lo + m) ^ fx) << 5) + 16384;
        const unsigned ta = t0 + hi * 8192, tb = t1 + hi * 8192;
#define RD_GUN(X, W, SB, TO, KS) do { _Pragma("unroll") for (int i = 0; i < 4; ++i) X[i] = ldf(sm + (SB) + (TO) + i * 2048); \
        _Pragma("unroll") for (int m = 0; m < 2; ++m) { W[m] = ldtr(sm + (SB) + xo[m] + (KS) * 4096); W[2 + m] = ldtr(sm + (SB) + xo[m] + 8192 + (KS) * 4096); } } while (0)
#define MM_GUN(X, W) do { _Pragma("unroll") for (int j = 0; j < 4; ++j) _Pragma("unroll") for (int i = 0; i < 4; ++i) acc[j][i] = mma(W[j], X[i], acc[j][i]); } while (0)
        bar();
        RD_GUN(xa, wa, (10 & 3) * SLOT_B, ta, 0);
#pragma unroll 1
        for (int n = 10; n < 20; ++n) {
          SCHED0;
          RD_GUN(xb, wb, (n & 3) * SLOT_B, tb, 1);
          MM_GUN(xa, wa);
          interleave<16, 12>();
          SCHED0;
          if (n == 11) waitv<0>();
          wait_lds(); bar();
          RD_GUN(xa, wa, ((n + 1) & 3) * SLOT_B, ta, 0);
          MM_GUN(xb, wb);
          interleave<16, 12>();
        }
        SCHED0;
        const int fg = 4 * mm + ct;
        const unsigned hbase = WS_H + ((rt * 2 + hi) * 12 + 8 + (fg >> 1)) * 16384 + ((fg & 1) * 2 + lo) * 4096 + l16;
#pragma unroll
        for (int i = 0; i < 4; ++i) {
          float z = *(const __attribute__((address_space(3))) float*)(sm + Z_OFF + (hi * 64 + i * 16 + r16) * 4);
          float c = fminf(1.0f + __builtin_amdgcn_exp2f(z * 1.44269504088896f), 1e38f);
          st16(pk8(glu4(acc[0][i], acc[2][i], c), glu4(acc[1][i], acc[3][i], c)), rws, hbase + i * 1024);
        }
      }
    }
    STAMP(3);
    f32x4 o[5];
    {
      f32x4 acc[5][4] = {};
      bf16x8 xa[4], wa[5], xb[4], wb[5];
      const unsigned ta = t0 + hi * 8192, tb = t1 + hi * 8192, ua = l16 + 16384 + lo * 10240, ub = ua + 1024;
#define MM_Y(X, W) do { _Pragma("unroll") for (int j = 0; j < 5; ++j) _Pragma("unroll") for (int i = 0; i < 4; ++i) acc[j][i] = mma(W[j], X[i], acc[j][i]); } while (0)
      bar();
      STAMP(4);
      RD_TT(xa, wa, (b2 & 3) * SLOT_B, ta, ua);
#pragma unroll 1
      for (int n = b2; n < b3; ++n) {
        SCHED0;
        RD_TT(xb, wb, (n & 3) * SLOT_B, tb, ub);
        MM_Y(xa, wa);
        interleave<20, 9>();
        SCHED0;
        if (n == b2 + 2) waitv<0>();
        wait_lds(); bar();
        RD_TT(xa, wa, ((n + 1) & 3) * SLOT_B, ta, ua);
        MM_Y(xb, wb);
        interleave<20, 9>();
      }
      SCHED0;
      STAMP(5);
#pragma unroll
      for (int i = 0; i < 4; ++i) {
        float z = *(const __attribute__((address_space(3))) float*)(sm + Z_OFF + (hi * 64 + i * 16 + r16) * 4);
        float g = __builtin_amdgcn_rcpf(1.0f + __builtin_amdgcn_exp2f(z * -1.44269504088896f));
#pragma unroll
        for (int j = 0; j < 5; ++j) acc[j][i] *= g;
      }
      if (wid == kq) {
#pragma unroll
        for (int i = 0; i < 4; ++i) {
          if (i == kq) {
#pragma unroll
            for (int j = 0; j < 5; ++j) o[j] = acc[j][i];
          } else {
            const unsigned d = SPARE + ((i - kq - 1) & 3) * 5120 + l16;
#pragma unroll
            for (int j = 0; j < 5; ++j) *(__attribute__((address_space(3))) f32x4*)(sm + d + j * 1024) = acc[j][i];
          }
        }
      } else {
        const unsigned sbase = WS_SL + (((rt * 4 + ct) * 4 + wid) * 3 + ((kq - wid - 1) & 3)) * 10240;
#pragma unroll
        for (int i = 0; i < 4; ++i) {
          st16(pk8(acc[0][i], acc[1][i]), rws, sbase + i * 2560 + l16);
          st16(pk8(acc[2][i], acc[3][i]), rws, sbase + i * 2560 + 1024 + l16);
          u32x2 v = {pk2(acc[4][i][0], acc[4][i][1]), pk2(acc[4][i][2], acc[4][i][3])};
          st8(v, rws, sbase + i * 2560 + 2048 + lane * 8);
        }
      }
    }
    {
      bf16x8 ha[2], wa[10], hb[2], wb[10];
      const unsigned ho = l16 + wid * 1024, wo = l16 + 16384;
#define RD_O(H, W, SB, HF) do { _Pragma("unroll") for (int k = 0; k < 2; ++k) { H[k] = ldf(sm + (SB) + ho + (2 * (HF) + k) * 4096); \
      _Pragma("unroll") for (int j = 0; j < 5; ++j) W[k * 5 + j] = ldf(sm + (SB) + wo + ((2 * (HF) + k) * 5 + j) * 1024); } } while (0)
#define MM_O(H, W) do { _Pragma("unroll") for (int k = 0; k < 2; ++k) _Pragma("unroll") for (int j = 0; j < 5; ++j) o[j] = mma(W[k * 5 + j], H[k], o[j]); } while (0)
      wait_lds(); bar();
      STAMP(6);
      if (wid != kq) {
        const unsigned d = SPARE + ((wid - kq - 1) & 3) * 5120 + l16;
#pragma unroll
        for (int j = 0; j < 5; ++j) o[j] = *(const __attribute__((address_space(3))) f32x4*)(sm + d + j * 1024);
      }
      RD_O(ha, wa, (b3 & 3) * SLOT_B, 0);
#pragma unroll 1
      for (int n = b3; n < b4; ++n) {
        SCHED0;
        RD_O(hb, wb, (n & 3) * SLOT_B, 1);
        MM_O(ha, wa);
        SCHED0;
        if (n == b3 + 2) waitv<0>();
        wait_lds(); bar();
        RD_O(ha, wa, ((n + 1) & 3) * SLOT_B, 0);
        MM_O(hb, wb);
      }
      SCHED0;
      STAMP(7);
      const unsigned sl = (b4 & 3) * SLOT_B + wid * 2560;
#pragma unroll
      for (int s = 0; s < 3; ++s) {
        u32x4 u = *(const __attribute__((address_space(3))) u32x4*)(sm + sl + s * 10240 + l16);
        u32x4 v = *(const __attribute__((address_space(3))) u32x4*)(sm + sl + s * 10240 + 1024 + l16);
        u32x2 w = *(const __attribute__((address_space(3))) u32x2*)(sm + sl + s * 10240 + 2048 + lane * 8);
        o[0] += (f32x4){bflo(u[0]), bfhi(u[0]), bflo(u[1]), bfhi(u[1])};
        o[1] += (f32x4){bflo(u[2]), bfhi(u[2]), bflo(u[3]), bfhi(u[3])};
        o[2] += (f32x4){bflo(v[0]), bfhi(v[0]), bflo(v[1]), bfhi(v[1])};
        o[3] += (f32x4){bflo(v[2]), bfhi(v[2]), bflo(v[3]), bfhi(v[3])};
        o[4] += (f32x4){bflo(w[0]), bfhi(w[0]), bflo(w[1]), bfhi(w[1])};
      }
      char* op = out + (size_t)(rt * 128 + mm * 64 + wid * 16 + r16) * (DM * 2) + (ct * 160 + odd * 80 + q * 4) * 2;
#pragma unroll
      for (int j = 0; j < 5; ++j) {
        u32x2 v = {pk2(o[j][0], o[j][1]), pk2(o[j][2], o[j][3])};
        *(u32x2*)(op + j * 32) = v;
      }
      STAMP(8);
    }
  } else if (wid < 7) {
    if (wid == 4) loader<0>(sm, x, mm ? ws_im : ws_re, mm ? wd_im : wd_re, wg, wu, rws, lane, rt, ct, kq, wsb);
    else if (wid == 5) loader<1>(sm, x, mm ? ws_im : ws_re, mm ? wd_im : wd_re, wg, wu, rws, lane, rt, ct, kq, wsb);
    else loader<2>(sm, x, mm ? ws_im : ws_re, mm ? wd_im : wd_re, wg, wu, rws, lane, rt, ct, kq, wsb);
  } else {
    volatile lds_u32* rdy = (volatile lds_u32*)(sm + RDY_OFF);
    __builtin_amdgcn_fence(__ATOMIC_ACQUIRE, "agent");
    if (lane == 0) *rdy = 0;
    auto dma4 = [&](unsigned voff, unsigned ldsb) {
      asm volatile("s_mov_b32 m0, %0\n\ts_nop 3\n\tbuffer_load_dword %1, %2, 0 offen sc1 lds" ::"s"(ldsb), "v"(voff), "s"(rws) : "memory");
    };
    if (lane == 0) dma4(WS_FLAG, PK_EP);
    const int unit = odd ? (rt < 8 ? 128 + (mm * 8 + rt) * 4 + ct : -1) : (mm * 16 + rt) * 4 + ct;
    const int ufb = unit >> 2, uct = unit & 3;
    if (unit >= 0) {
      const rsrc_t rdn = mk_rsrc(wdn, FF * DM * 2);
#pragma unroll
      for (int p = 0; p < 10; ++p) {
        const int c = p * 64 + lane, row = c / 20, ch = c - row * 20;
        dma1(rdn, row * (DM * 2) + ch * 16, ufb * 32 * (DM * 2) + uct * 320, SPARE + p * 1024);
      }
    }
    {
      unsigned t = 0;
#pragma unroll
      for (int i = 0; i < 20; ++i) t ^= gate_w[i * 16];
      asm volatile("" ::"s"(t));
    }
    wait_lds();
    bar();
    float z0 = 0.f, z1 = 0.f;
    unsigned token = 0;
    const unsigned zo = lane * 128, zs = (lane & 6) << 4;
    const unsigned fa = FL_AB + (mm * 4 + ct) * 128 + (odd ? max(lane & 15, N3A >> 1) : min(lane & 15, (N3A - 1) >> 1)) * 4;
    const unsigned fh = lane < 16 ? FL_H + rt * 128 + lane * 4 : FL_WD + ct * 256 + (lane - 16) * 4;
    const unsigned fs = FL_SL + (rt * 4 + ct) * 128 + (((lane & 3) == kq ? kq + 1 : lane) & 3) * 4;
    int lvl = 0;
#define PEEK(L, FOFF, NL, LOFF) do { if (lvl < (L)) { \
      const unsigned v_ = *(volatile lds_u32*)(sm + (LOFF) + (lane & ((NL) - 1)) * 4); \
      if (__all(v_ == token)) { lvl = (L); if (lane == 0) *rdy = (L); } \
      else if (lane < (NL)) dma4((FOFF), (LOFF)); } } while (0)
#define AWAIT(L, FOFF) do { if (lvl < (L)) { \
      for (unsigned spin = 0; spin < SPIN_MAX; ++spin) { \
        if (__all(__hip_atomic_load((const gu32*)(wsb + (FOFF)), __ATOMIC_RELAXED, __HIP_MEMORY_SCOPE_AGENT) == token)) break; \
        __builtin_amdgcn_s_sleep(1); } \
      lvl = (L); if (lane == 0) *rdy = (L); } } while (0)
#define PUBLISH(FOFF) do { if (lane == 0) __hip_atomic_store((gu32*)(wsb + (FOFF)), token, __ATOMIC_RELAXED, __HIP_MEMORY_SCOPE_AGENT); } while (0)
#pragma unroll 1
    for (int n = 0; n < 10; ++n) {
      const unsigned sb = odd ? (n & 3) * SLOT_B : (n % 3) * SLOT_A;
      const unsigned* gw = gate_w + n * 32;
#pragma unroll
      for (int c = 0; c < 8; ++c) {
        u32x4 u = *(const __attribute__((address_space(3))) u32x4*)(sm + sb + zo + ((c << 4) ^ zs));
        u32x4 v = *(const __attribute__((address_space(3))) u32x4*)(sm + sb + 8192 + zo + ((c << 4) ^ zs));
#pragma unroll
        for (int j = 0; j < 4; ++j) {
          bf16x2 g2 = __builtin_bit_cast(bf16x2, gw[c * 4 + j]);
          const unsigned uj = u[j], vj = v[j];
          z0 = __builtin_amdgcn_fdot2_f32_bf16(__builtin_bit_cast(bf16x2, uj), g2, z0, false);
          z1 = __builtin_amdgcn_fdot2_f32_bf16(__builtin_bit_cast(bf16x2, vj), g2, z1, false);
        }
      }
      asm volatile("" : "+v"(z0), "+v"(z1));
      if (n == 5) {
        waitv<0>();
        token = __builtin_amdgcn_readfirstlane(*(volatile lds_u32*)(sm + PK_EP)) + 0x9E3779B9u;
        *(volatile lds_u32*)(sm + PK_H + lane * 4) = ~token;
        if (lane < 32) *(volatile lds_u32*)(sm + PK_AB + lane * 4) = ~token;
        if (unit >= 0) {
          const int g = lane >> 4, qq = (lane & 15) >> 2;
          const unsigned ro = SPARE + (4 * g + qq) * 320 + (lane & 3) * 8;
          const unsigned wb = WS_WD + ((uct * 2) * 12 + (ufb >> 2)) * 20480 + (ufb & 3) * 5120 + lane * 16;
#pragma unroll
          for (int e = 0; e < 10; ++e) {
            s16x4 lo = __builtin_amdgcn_ds_read_tr16_b64_v4i16((__attribute__((address_space(3))) s16x4*)(sm + ro + e * 32));
            s16x4 hi = __builtin_amdgcn_ds_read_tr16_b64_v4i16((__attribute__((address_space(3))) s16x4*)(sm + ro + e * 32 + 16 * 320));
            s16x8 v = __builtin_shufflevector(lo, hi, 0, 1, 2, 3, 4, 5, 6, 7);
            st16(__builtin_bit_cast(u32x4, v), rws, wb + (e / 5) * (12 * 20480) + (e % 5) * 1024);
          }
        }
      }
      if (n == 9) {
        const int r0 = lane, r1 = lane + 64;
        const int p0 = odd ? (32 * (r0 >> 5) + 8 * ((r0 & 15) >> 2) + 4 * ((r0 >> 4) & 1) + (r0 & 3)) : r0;
        const int p1 = odd ? (32 * (r1 >> 5) + 8 * ((r1 & 15) >> 2) + 4 * ((r1 >> 4) & 1) + (r1 & 3)) : r1;
        *(__attribute__((address_space(3))) float*)(sm + Z_OFF + p0 * 4) = z0;
        *(__attribute__((address_space(3))) float*)(sm + Z_OFF + p1 * 4) = z1;
      }
      if (!odd && n >= 6) PEEK(1, fa, 16, PK_AB);
      wait_lds();
      bar();
    }
    if (odd) {
      bar();
#pragma unroll 1
      for (int n = 10; n < 20; ++n) {
        if (n == 12) PUBLISH(FL_AB + (mm * 4 + ct) * 128 + rt * 4);
        if (n >= 13) PEEK(1, fa, 16, PK_AB);
        if (n == 17) AWAIT(1, fa);
        wait_lds();
        bar();
      }
    }
    AWAIT(1, fa);
    wait_lds();
    STAMP(1);
    bar();
#pragma unroll 1
    for (int n = b2; n < b3; ++n) {
      if (n == b2 + 3) {
        PUBLISH(FL_H + rt * 128 + (ct * 4 + kq) * 4);
        waitv<0>();
        if (unit >= 0) PUBLISH(FL_WD + uct * 256 + ufb * 4);
      }
      if (n >= b3 - 8) PEEK(2, fh, 64, PK_H);
      if (n == b3 - 3) AWAIT(2, fh);
      wait_lds();
      bar();
    }
    STAMP(2);
    bar();
#pragma unroll 1
    for (int n = b3; n < b4; ++n) {
      if (n == b3 + 3) PUBLISH(FL_SL + (rt * 4 + ct) * 128 + kq * 4);
      if (n >= b3 + 4) PEEK(3, fs, 4, PK_SL);
      if (n == b4 - 1) AWAIT(3, fs);
      wait_lds();
      bar();
    }
    STAMP(3);
    if (bid == 0) {
      for (unsigned spin = 0; spin < SPIN_MAX; ++spin) {
        u32x4 v = __builtin_amdgcn_raw_buffer_load_b128(rws, FL_H + (lane >> 2) * 128 + (lane & 3) * 16, 0, 16);
        if (__all(v[0] == token && v[1] == token && v[2] == token && v[3] == token)) break;
        __builtin_amdgcn_s_sleep(1);
      }
      PUBLISH(WS_FLAG);
    }
    waitv<0>();
  }
}

extern "C" void kernel_launch(void* const* d_in, const int* in_sizes, int n_in, void* d_out, int out_size, void* d_ws, size_t ws_size,
                              hipStream_t stream) {
  (void)in_sizes; (void)n_in; (void)out_size; (void)ws_size;
  (void)hipFuncSetAttribute((const void*)fused_kernel, hipFuncAttributeMaxDynamicSharedMemorySize, LDS_BYTES);
  fused_kernel<<<256, 512, LDS_BYTES, stream>>>((const char*)d_in[0], (const char*)d_in[1], (const char*)d_in[2], (const char*)d_in[3],
                                                (const char*)d_in[4], (const char*)d_in[6], (const char*)d_in[7], (const char*)d_in[8],
                                                (const unsigned*)d_in[9], (char*)d_out, (char*)d_ws);
}
```

```cpp
#include <hip/hip_runtime.h>
#include <hip/hip_bf16.h>
#include <stdint.h>

typedef __attribute__((ext_vector_type(8))) __bf16 bf16x8;
typedef __attribute__((ext_vector_type(2))) __bf16 bf16x2;
typedef __attribute__((ext_vector_type(4))) short s16x4;
typedef __attribute__((ext_vector_type(8))) short s16x8;
typedef __attribute__((ext_vector_type(4))) float f32x4;
typedef __attribute__((ext_vector_type(2))) float f32x2;
typedef __attribute__((ext_vector_type(4))) unsigned u32x4;
typedef __attribute__((ext_vector_type(2))) unsigned u32x2;
typedef __attribute__((address_space(3))) char* lds_p;
typedef __attribute__((address_space(1))) unsigned gu32;
typedef __attribute__((address_space(3))) unsigned lds_u32;
typedef __amdgpu_buffer_rsrc_t rsrc_t;

#define DEV __device__ __forceinline__

constexpr int SEQ = 2048, DM = 640, FF = 1536;
#define N3A 17
#define NS 4
constexpr int NT = 12 - 2 * NS;
constexpr int SLOT_A = 49152, SLOT_B = 36864;
constexpr int SPARE = 147456;
constexpr int GW_OFF = SPARE + 10240;
constexpr int Z_OFF = SPARE + 15360;
constexpr int PK_H = SPARE + 15872;
constexpr int PK_AB = SPARE + 16128, PK_SL = SPARE + 16192, RDY_OFF = SPARE + 16208, PK_EP = SPARE + 16212, TOK_OFF = SPARE + 16216;
constexpr int LDS_BYTES = 163840;
constexpr unsigned WS_FLAG = 0;
constexpr unsigned FL_AB = 128, FL_H = 2048, FL_WD = 4096, FL_SL = 8192;
constexpr unsigned WS_Z = 16384;
constexpr unsigned WS_AB = WS_Z + 65536;
constexpr unsigned WS_H1 = WS_AB + 2 * 4 * 32 * 20480;
constexpr unsigned WS_H2 = WS_H1 + 16 * 4 * NS * 16384;
constexpr unsigned WS_W1 = WS_H2 + 32 * NT * 16384;
constexpr unsigned WS_W2 = WS_W1 + 4 * 4 * NS * 20480;
constexpr unsigned WS_SL = WS_W2 + 8 * NT * 20480;
constexpr unsigned WS_END = WS_SL + 256 * 30720;
constexpr unsigned SPIN_MAX = 200000;
constexpr float LOG2E = 1.44269504088896f;

template <int N> DEV void waitv() { asm volatile("s_waitcnt vmcnt(%0)" ::"n"(N) : "memory"); }
DEV void bar() {
  asm volatile("" ::: "memory");
  __builtin_amdgcn_s_barrier();
  asm volatile("" ::: "memory");
}
DEV void wait_lds() { __builtin_amdgcn_s_waitcnt(0xc07f); }
#define SCHED0 __builtin_amdgcn_sched_barrier(0)

DEV void dma1(rsrc_t r, unsigned voff, unsigned soff, unsigned ldsb) {
  asm volatile("s_mov_b32 m0, %0\n\ts_nop 3\n\tbuffer_load_dwordx4 %1, %2, %3 offen sc1 lds" ::"s"(ldsb), "v"(voff), "s"(r), "s"(soff) : "memory");
}
#define SCX " sc1"
DEV void dma4x(rsrc_t r, const unsigned (&v)[4], unsigned soff, unsigned ldsb) {
  asm volatile("s_mov_b32 m0, %0\n\ts_nop 0\n\t"
               "buffer_load_dwordx4 %1, %5, %6 offen" SCX " lds\n\tbuffer_load_dwordx4 %2, %5, %6 offen offset:1024" SCX " lds\n\t"
               "buffer_load_dwordx4 %3, %5, %6 offen offset:2048" SCX " lds\n\tbuffer_load_dwordx4 %4, %5, %6 offen offset:3072" SCX " lds"
               ::"s"(ldsb), "v"(v[0]), "v"(v[1]), "v"(v[2]), "v"(v[3]), "s"(r), "s"(soff) : "memory");
}
DEV void dmaw(rsrc_t r, unsigned voff, unsigned ldsb) {
  asm volatile("s_mov_b32 m0, %0\n\ts_nop 3\n\tbuffer_load_dword %1, %2, 0 offen sc1 lds" ::"s"(ldsb), "v"(voff), "s"(r) : "memory");
}
DEV void warm(const char* p) {
  asm volatile("s_mov_b32 m0, %0\n\ts_nop 0\n\tglobal_load_lds_dword %1, off sc1" ::"s"(PK_H), "v"(p) : "memory");
}
DEV rsrc_t mk_rsrc(const void* p, unsigned bytes) { return __builtin_amdgcn_make_buffer_rsrc((void*)p, (short)0, (int)bytes, 0x00020000); }
DEV bf16x8 ldf(unsigned o) { return *(const __attribute__((address_space(3))) bf16x8*)(size_t)o; }
DEV unsigned vadd(unsigned s, unsigned v) { unsigned r; asm volatile("v_add_u32 %0, %1, %2" : "=v"(r) : "s"(s), "v"(v)); return r; }
DEV bf16x8 ldtr(unsigned o) {
  s16x4 lo = __builtin_amdgcn_ds_read_tr16_b64_v4i16((__attribute__((address_space(3))) s16x4*)(size_t)o);
  s16x4 hi = __builtin_amdgcn_ds_read_tr16_b64_v4i16((__attribute__((address_space(3))) s16x4*)(size_t)(o + 512));
  s16x8 v = __builtin_shufflevector(lo, hi, 0, 1, 2, 3, 4, 5, 6, 7);
  return __builtin_bit_cast(bf16x8, v);
}
DEV f32x4 mma(bf16x8 a, bf16x8 b, f32x4 c) { return __builtin_amdgcn_mfma_f32_16x16x32_bf16(a, b, c, 0, 0, 0); }
DEV unsigned pk2(float a, float b) { f32x2 v = {a, b}; return __builtin_bit_cast(unsigned, __builtin_convertvector(v, bf16x2)); }
DEV u32x4 pk8(f32x4 a, f32x4 b) { u32x4 r = {pk2(a[0], a[1]), pk2(a[2], a[3]), pk2(b[0], b[1]), pk2(b[2], b[3])}; return r; }
DEV float bflo(unsigned v) { return __builtin_bit_cast(float, v << 16); }
DEV float bfhi(unsigned v) { return __builtin_bit_cast(float, v & 0xffff0000u); }
DEV void st16(u32x4 v, rsrc_t r, unsigned off, unsigned soff) { __builtin_amdgcn_raw_buffer_store_b128(v, r, off, soff, 16); }
DEV void st8(u32x2 v, rsrc_t r, unsigned off, unsigned soff) { __builtin_amdgcn_raw_buffer_store_b64(v, r, off, soff, 16); }
template <int NM, int NR> DEV void interleave() {
#pragma unroll
  for (int i = 0; i < NR; ++i) {
    __builtin_amdgcn_sched_group_barrier(0x008, 1, 0);
    __builtin_amdgcn_sched_group_barrier(0x100, 1, 0);
  }
  if constexpr (NM > NR) __builtin_amdgcn_sched_group_barrier(0x008, NM - NR, 0);
}
#define STAMP(i) do {} while (0)
#define DST(i) do {} while (0)

DEV f32x4 glu4(f32x4 G, f32x4 U) {
  f32x4 r;
#pragma unroll
  for (int j = 0; j < 4; ++j) r[j] = U[j] * __builtin_amdgcn_rcpf(1.0f + __builtin_amdgcn_exp2f(G[j] * -LOG2E));
  return r;
}
DEV float expz(lds_p sm, int row) {
  const float z = *(const __attribute__((address_space(3))) float*)(sm + Z_OFF + row * 4);
  return __builtin_amdgcn_exp2f(fminf(fmaxf(z, -60.0f), 60.0f) * LOG2E);
}
DEV int rdy_now() {
  int s; unsigned v;
  asm volatile("ds_read_b32 %1, %2\n\ts_waitcnt lgkmcnt(0)\n\tv_readfirstlane_b32 %0, %1" : "=s"(s), "=&v"(v) : "v"(RDY_OFF) : "memory");
  return s;
}
template <int LVL> DEV void wait_rdy() {
  unsigned s, c, v;
  asm volatile("s_mov_b32 %1, %4\n"
               "1:\n\tds_read_b32 %2, %3\n\ts_waitcnt lgkmcnt(0)\n\tv_readfirstlane_b32 %0, %2\n\ts_cmp_ge_i32 %0, %5\n\ts_cbranch_scc1 2f\n\t"
               "s_sleep 1\n\ts_sub_u32 %1, %1, 1\n\ts_cbranch_scc0 1b\n2:"
               : "=&s"(s), "=&s"(c), "=&v"(v) : "v"(RDY_OFF), "n"(SPIN_MAX), "n"(LVL) : "memory", "scc");
}
DEV unsigned nexts(unsigned sb) { return sb == 3 * SLOT_B ? 0u : sb + SLOT_B; }
DEV unsigned nexta(unsigned sb) { return sb == 2 * SLOT_A ? 0u : sb + SLOT_A; }

template <int LW, unsigned GA, unsigned GB, bool FIVE = true>
DEV void issue(rsrc_t ra, const unsigned (&va)[4], unsigned sa, rsrc_t rb, const unsigned (&vb)[4], unsigned sc, unsigned sb) {
  if constexpr (LW == 0) {
#pragma unroll
    for (int g = 0; g < 3; ++g) dma4x(ra, va, sa + g * GA, sb + g * 4096);
  } else if constexpr (LW == 1) {
    dma4x(ra, va, sa + 3 * GA, sb + 3 * 4096);
#pragma unroll
    for (int g = 0; g < 2; ++g) dma4x(rb, vb, sc + g * GB, sb + 16384 + g * 4096);
  } else {
#pragma unroll
    for (int g = 2; g < (FIVE ? 5 : 4); ++g) dma4x(rb, vb, sc + g * GB, sb + 16384 + g * 4096);
  }
}
template <int LW> DEV void loader(const char* x, const char* wsq, const char* wd, const char* wg, const char* wu, rsrc_t rws,
                                  int lane, int rt, int ct, int kq) {
  const int odd = kq & 1, mm = kq >> 1, fg = 4 * mm + ct;
  const int lr = lane >> 3, lp = lane & 7;
  const unsigned chT = (lp ^ (lr & 6)) * 16;
  unsigned sb = 0;
  {
    unsigned vx[4], vw[4], vp[4];
#pragma unroll
    for (int j = 0; j < 4; ++j) {
      vx[j] = (lr + 8 * j) * (DM * 2) + chT - j * 1024;
      vw[j] = (lr + 8 * j) * (FF * 2) + (((lp >> 1) ^ (((lr >> 1) & 1) | ((j & 1) << 1))) * 2 + (lp & 1)) * 16 - j * 1024;
      vp[j] = (8 * (lane >> 5) + (lr & 3) + 16 * (j & 1) + 4 * (j >> 1)) * (DM * 2) + chT - j * 1024;
    }
    const rsrc_t rx = mk_rsrc(x, SEQ * DM * 2);
    const rsrc_t rw = mk_rsrc(LW == 2 ? wu : wg, DM * FF * 2);
    if (!odd) {
      unsigned so = LW == 0 ? rt * 128 * (DM * 2) : fg * 256;
      sb = 2 * SLOT_A;
#pragma unroll 1
      for (int j = 0; j < 10; ++j) {
        if (j == 3) { waitv<32>(); bar(); }
        if (j >= 3) { waitv<16>(); bar(); }
        if constexpr (LW == 0) {
#pragma unroll
          for (int g = 0; g < 4; ++g) dma4x(rx, vx, so + g * 32 * (DM * 2), sb + g * 4096);
          so += 128;
        } else {
#pragma unroll
          for (int g = 0; g < 4; ++g) dma4x(rw, vw, so + (g & 1) * 32 * (FF * 2) + (g >> 1) * 128, sb + LW * 16384 + g * 4096);
          so += 64 * (FF * 2);
        }
        sb = nexta(sb);
      }
      waitv<16>(); bar();
      sb = 0;
    } else {
      const rsrc_t rd = mk_rsrc(wd, DM * DM * 2);
      unsigned sa = rt * 128 * (DM * 2), sc = ct * 160 * (DM * 2);
#pragma unroll 1
      for (int j = 0; j < 10; ++j) {
        if (j == 4) { waitv<36>(); bar(); }
        if (j >= 4) { waitv<24>(); bar(); }
        issue<LW, 32 * (DM * 2), 32 * (DM * 2)>(rx, vp, sa, rd, vx, sc, sb);
        sa += 128; sc += 128; sb = nexts(sb);
      }
      sa = rt * 128 * (DM * 2); sc = 2048 + fg * 128 - (LW == 2 ? 64 * (FF * 2) : 0);
#pragma unroll 1
      for (int j = 10; j < 20; ++j) {
        waitv<LW == 2 ? 16 : 24>();
        bar();
        issue<LW, 32 * (DM * 2), 32 * (FF * 2), false>(rx, vx, sa, rw, vw, sc, sb);
        sa += 128; sc += 64 * (FF * 2); sb = nexts(sb);
      }
    }
  }
  const int n3 = odd ? 32 - N3A : N3A, s30 = odd ? N3A : 0;
  const unsigned vl[4] = {lane * 16u, lane * 16u, lane * 16u, lane * 16u};
  {
    unsigned vs[4];
#pragma unroll
    for (int k = 0; k < 4; ++k) vs[k] = (lr + 8 * k) * (SEQ * 2) + chT - k * 1024;
    const rsrc_t rq = mk_rsrc(wsq, SEQ * SEQ * 2);
    unsigned sa = rt * 128 * (SEQ * 2) + s30 * 128, sc = WS_AB + ((mm * 4 + ct) * 32 + s30) * 20480;
    int pb = odd ? 0 : 2;
#pragma unroll 1
    for (int j = 0; j < n3; ++j) {
      if (odd) {
        if (LW == 2 && j < 2) waitv<16>(); else waitv<24>();
        bar();
      } else if (j >= 4) {
        if (j == 4) { waitv<36>(); bar(); }
        waitv<24>(); bar();
      } else {
        while (pb > 0 && (j >= 3 - pb || rdy_now() < 1)) {
          if (pb == 2) { if (j > 0) waitv<12>(); else waitv<0>(); }
          bar(); --pb;
        }
      }
      if (j == 0) wait_rdy<1>();
      issue<LW, 32 * (SEQ * 2), 4096>(rq, vs, sa, rws, vl, sc, sb);
      sa += 128; sc += 20480; sb = nexts(sb);
    }
  }
  {
    unsigned sa = WS_H1 + (rt * 4 + kq) * NS * 16384, sc = WS_W1 + (ct * 4 + kq) * NS * 20480;
#pragma unroll 1
    for (int j = 0; j < NS + NT; ++j) {
      waitv<24>(); bar();
      if (j == 0) wait_rdy<2>();
      if (j == NS) { sa = WS_H2 + (rt * 2 + mm) * NT * 16384; sc = WS_W2 + (ct * 2 + odd) * NT * 20480; }
      issue<LW, 4096, 4096>(rws, vl, sa, rws, vl, sc, sb);
      sa += 16384; sc += 20480; sb = nexts(sb);
    }
  }
  waitv<24>(); bar();
  waitv<12>(); bar();
  waitv<0>(); bar();
  bar();
}

__global__ __launch_bounds__(512, 2) void fused_kernel(const char* __restrict__ x, const char* __restrict__ ws_re, const char* __restrict__ ws_im,
                                                       const char* __restrict__ wd_re, const char* __restrict__ wd_im,
                                                       const char* __restrict__ wg, const char* __restrict__ wu, const char* __restrict__ wdn,
                                                       const char* __restrict__ gate_w, char* __restrict__ out, char* wsb) {
  extern __shared__ __attribute__((aligned(16))) char smem_raw[];
  const lds_p sm = (lds_p)smem_raw;
  const int wid = __builtin_amdgcn_readfirstlane(threadIdx.x >> 6), lane = threadIdx.x & 63;
  const int bid = blockIdx.x, xcd = bid & 7, idx = bid >> 3;
  const int kq = xcd >> 1, rt = (xcd & 1) * 8 + (idx >> 2), ct = idx & 3;
  const int odd = kq & 1, mm = kq >> 1, fg = 4 * mm + ct;
  const int b2 = odd ? 20 : 10, n3 = odd ? 32 - N3A : N3A, s30 = odd ? N3A : 0;
  const int b3 = b2 + n3, b35 = b3 + NS, b4 = b35 + NT;
  constexpr int HP = 4;
  const rsrc_t rws = mk_rsrc(wsb, WS_END);
  STAMP(0);

  if (wid < 4) {
    const int r16 = lane & 15, q = lane >> 4;
    const unsigned t0 = r16 * 128 + ((q ^ (r16 & 6)) << 4), t1 = t0 ^ 64;
    const int q4 = r16 >> 2, fx = ((q4 >> 1) & 1) | ((q & 1) << 1);
    const unsigned xrow = (8 * q + q4) * 128 + (lane & 3) * 8;
    const int hi = wid >> 1, lo = wid & 1;
    const unsigned l16 = lane * 16;
    if (!odd) {
      f32x4 acc[8][4] = {};
      bf16x8 xa[4], wa[8], xb[4], wb[8];
      unsigned xo[4], po[4];
#pragma unroll
      for (int m = 0; m < 4; ++m) { xo[m] = xrow + ((m ^ fx) << 5) + 16384 + lo * 8192; po[m] = xo[m] + 2 * SLOT_A; }
      const unsigned ta = t0 + hi * 8192, tb = t1 + hi * 8192;
      unsigned pa = ta + 2 * SLOT_A, pb = tb + 2 * SLOT_A;
#define RD_GUW(X, W, TP, KS) do { _Pragma("unroll") for (int i = 0; i < 4; ++i) X[i] = ldf((TP) + i * 2048); \
      _Pragma("unroll") for (int m = 0; m < 4; ++m) { W[m] = ldtr(po[m] + (KS) * 4096); W[4 + m] = ldtr(po[m] + 16384 + (KS) * 4096); } } while (0)
#define MM_GUW(X, W, K0, K1) do { _Pragma("unroll") for (int k = (K0); k < (K1); ++k) acc[k >> 2][k & 3] = mma(W[k >> 2], X[k & 3], acc[k >> 2][k & 3]); } while (0)
      bar();
      STAMP(1);
      unsigned sb = 2 * SLOT_A;
      RD_GUW(xa, wa, pa, 0);
#pragma unroll 1
      for (int n = 0; n < 10; ++n) {
        SCHED0;
        RD_GUW(xb, wb, pb, 1);
        MM_GUW(xa, wa, 0, 21);
        interleave<21, 20>();
        SCHED0;
        sb = nexta(sb);
#pragma unroll
        for (int m = 0; m < 4; ++m) { MM_GUW(xa, wa, 21 + m, 22 + m); SCHED0; po[m] = vadd(sb, xo[m]); SCHED0; }
        MM_GUW(xa, wa, 25, 26); SCHED0; pa = vadd(sb, ta); SCHED0;
        MM_GUW(xa, wa, 26, 27); SCHED0; pb = vadd(sb, tb); SCHED0;
        MM_GUW(xa, wa, 27, 32);
        SCHED0;
        wait_lds(); bar(); DST(n);
        RD_GUW(xa, wa, pa, 0);
        MM_GUW(xb, wb, 0, 32);
        interleave<32, 20>();
      }
      SCHED0;
      STAMP(2);
      const bool h1 = fg < 2 * NS;
      const unsigned hbase = h1 ? WS_H1 + ((rt * 4 * NS + fg * 2 + lo) * 16 + hi * 8) * 1024
                                : WS_H2 + (((rt * 2 + hi) * NT + fg - 2 * NS) * 16 + lo * 8) * 1024;
      const unsigned sp = h1 ? 1024 : 4096, si = h1 ? 2048 : 1024;
#pragma unroll
      for (int i = 0; i < 4; ++i)
#pragma unroll
        for (int p = 0; p < 2; ++p)
          st16(pk8(glu4(acc[2 * p][i], acc[4 + 2 * p][i]), glu4(acc[2 * p + 1][i], acc[5 + 2 * p][i])), rws, l16, hbase + p * sp + i * si);
    } else {
      unsigned sb = 0;
      {
        f32x4 acc[4][5] = {};
        bf16x8 xa[4], wa[5], xb[4], wb[5];
        const unsigned ta = t0 + hi * 8192, ua = t0 + 16384 + lo * 10240;
        unsigned pa = ta, pu = ua;
#define RD_TT(X, W, TP, UP) do { _Pragma("unroll") for (int i = 0; i < 4; ++i) X[i] = ldf((TP) + i * 2048); \
        _Pragma("unroll") for (int j = 0; j < 5; ++j) W[j] = ldf((UP) + j * 2048); } while (0)
#define MM_AB(X, W, K0, K1) do { _Pragma("unroll") for (int k = (K0); k < (K1); ++k) acc[k & 3][k >> 2] = mma(X[k & 3], W[k >> 2], acc[k & 3][k >> 2]); } while (0)
        bar();
        STAMP(1);
        RD_TT(xa, wa, pa, pu);
        unsigned pb = pa ^ 64, pv = pu ^ 64;
#pragma unroll 1
        for (int n = 0; n < 10; ++n) {
          SCHED0;
          RD_TT(xb, wb, pb, pv);
          MM_AB(xa, wa, 0, 11);
          interleave<11, 9>();
          SCHED0;
          sb = nexts(sb);
          MM_AB(xa, wa, 11, 12); SCHED0; pa = vadd(sb, ta); SCHED0;
          MM_AB(xa, wa, 12, 13); SCHED0; pu = vadd(sb, ua); SCHED0;
          MM_AB(xa, wa, 13, 14); SCHED0; pb = vadd(sb, ta ^ 64); SCHED0;
          MM_AB(xa, wa, 14, 15); SCHED0; pv = vadd(sb, ua ^ 64); SCHED0;
          MM_AB(xa, wa, 15, 20);
          SCHED0;
          wait_lds(); bar(); DST(n);
          RD_TT(xa, wa, pa, pu);
          MM_AB(xb, wb, 0, 20);
          interleave<20, 9>();
        }
        SCHED0;
        const unsigned abase = WS_AB + ((mm * 4 + ct) * 32 + rt * 2 + hi) * 20480 + lo * 10240;
        const unsigned sg = mm ? 0x80008000u : 0u;
#pragma unroll
        for (int j = 0; j < 5; ++j)
#pragma unroll
          for (int p = 0; p < 2; ++p) {
            u32x4 v = pk8(acc[2 * p][j], acc[2 * p + 1][j]);
            v ^= sg;
            st16(v, rws, l16 + j * 2048 + p * 1024, abase);
          }
      }
      STAMP(2);
      {
        f32x4 acc[4][4] = {}, zc[2] = {};
        bf16x8 xa[4], wa[5], xb[4], wb[5];
        unsigned xo[2], po[2];
#pragma unroll
        for (int m = 0; m < 2; ++m) { xo[m] = xrow + (((2 * lo + m) ^ fx) << 5) + 16384; po[m] = xo[m] + sb; }
        const unsigned ta = t0 + hi * 8192 + lo * 4096, tc = ta ^ 4096;
        unsigned pa = ta + sb, pb = (ta ^ 64) + sb, pc = tc + sb, pd = (tc ^ 64) + sb, go = GW_OFF + q * 16;
#define RD_GUN(X, W, TP, TQ, KS) do { X[0] = ldf(TP); X[1] = ldf((TP) + 2048); X[2] = ldf(TQ); X[3] = ldf((TQ) + 2048); W[4] = ldf(go + (KS) * 64); \
        _Pragma("unroll") for (int m = 0; m < 2; ++m) { W[m] = ldtr(po[m] + (KS) * 4096); W[2 + m] = ldtr(po[m] + 8192 + (KS) * 4096); } } while (0)
#define MM_GUN(X, W, K0, K1) do { _Pragma("unroll") for (int k = (K0); k < (K1); ++k) { \
        if (k < 16) acc[k >> 2][k & 3] = mma(W[k >> 2], X[k & 3], acc[k >> 2][k & 3]); else zc[k & 1] = mma(W[4], X[k & 1], zc[k & 1]); } } while (0)
        RD_GUN(xa, wa, pa, pc, 0);
#pragma unroll 1
        for (int pass = 0; pass < 2; ++pass) {
#pragma unroll 1
          for (int n = pass ? 9 : 1; n > 0; --n) {
            SCHED0;
            RD_GUN(xb, wb, pb, pd, 1);
            MM_GUN(xa, wa, 0, 13);
            interleave<13, 13>();
            SCHED0;
            sb = nexts(sb);
            MM_GUN(xa, wa, 13, 14); SCHED0; pa = vadd(sb, ta); pc = vadd(sb, tc); SCHED0;
            MM_GUN(xa, wa, 14, 15); SCHED0; po[0] = vadd(sb, xo[0]); po[1] = vadd(sb, xo[1]); SCHED0;
            MM_GUN(xa, wa, 15, 16); SCHED0; pb = vadd(sb, ta ^ 64); pd = vadd(sb, tc ^ 64); SCHED0;
            MM_GUN(xa, wa, 16, 17); SCHED0; go = vadd(128, go); SCHED0;
            MM_GUN(xa, wa, 17, 18);
            SCHED0;
            wait_lds(); bar(); DST(pass ? 20 - n : 11 - n);
            RD_GUN(xa, wa, pa, pc, 0);
            MM_GUN(xb, wb, 0, 18);
            interleave<18, 13>();
          }
          SCHED0;
          waitv<0>();
        }
        if (q == 0) {
          const unsigned zr = (hi * 64 + lo * 32 + r16) * 4;
#pragma unroll
          for (int u = 0; u < 2; ++u) {
            *(__attribute__((address_space(3))) float*)(sm + Z_OFF + zr + u * 64) = zc[u][0];
            __builtin_amdgcn_raw_buffer_store_b32(__builtin_bit_cast(unsigned, zc[u][0]), rws, zr + u * 64, WS_Z + ((rt * 4 + ct) * 2 + mm) * 512, 16);
          }
        }
        const int fb = 32 + fg * 2 + lo - 8 * NS;
        const unsigned hbase = WS_H2 + (((rt * 2 + hi) * NT + (fb >> 2)) * 16 + (fb & 3) * 4) * 1024;
#pragma unroll
        for (int i = 0; i < 4; ++i)
          st16(pk8(glu4(acc[0][i], acc[2][i]), glu4(acc[1][i], acc[3][i])), rws, l16 + (i & 1) * 1024, hbase + ((i >> 1) ^ lo) * 2048);
      }
    }
    STAMP(3);
    unsigned sb = 0;
    {
      f32x4 acc[5][4] = {};
      bf16x8 xa[4], wa[5], xb[4], wb[5];
      unsigned ta = t0 + hi * 8192, tb = t1 + hi * 8192;
      const unsigned ua = l16 + 16384 + lo * 10240;
      unsigned pa = ta, pb = tb, pu = ua, pv = ua;
#define MM_Y(X, W, K0, K1) do { _Pragma("unroll") for (int k = (K0); k < (K1); ++k) acc[k >> 2][k & 3] = mma(W[k >> 2], X[k & 3], acc[k >> 2][k & 3]); } while (0)
      if (!odd) bar();
      STAMP(4);
      RD_TT(xa, wa, pa, pu);
      int dn = b2;
#pragma unroll 1
      for (int pass = 0; pass < 3; ++pass) {
#pragma unroll 1
        for (int n = pass == 0 ? HP : pass == 1 ? n3 - HP : NS; n > 0; --n) {
          SCHED0;
          RD_TT(xb, wb, pb, pv + 1024);
          MM_Y(xa, wa, 0, 11);
          interleave<11, 9>();
          SCHED0;
          sb = nexts(sb);
          MM_Y(xa, wa, 11, 12); SCHED0; pa = vadd(sb, ta); SCHED0;
          MM_Y(xa, wa, 12, 13); SCHED0; pu = vadd(sb, ua); SCHED0;
          MM_Y(xa, wa, 13, 14); SCHED0; pb = vadd(sb, tb); SCHED0;
          MM_Y(xa, wa, 14, 20);
          SCHED0;
          wait_lds(); bar(); DST(dn); ++dn;
          RD_TT(xa, wa, pa, pu);
          MM_Y(xb, wb, 0, 20);
          interleave<20, 9>();
          pv = pu;
        }
        SCHED0;
        if (pass == 0) waitv<0>();
        if (pass == 1) {
          STAMP(5);
          ta = l16 + hi * 8192; tb = ta + 1024;
          pa = ta + sb; pb = tb + sb;
#pragma unroll
          for (int i = 0; i < 4; ++i) xa[i] = ldf(pa + i * 2048);
#pragma unroll
          for (int i = 0; i < 4; ++i) {
            const float ez = expz(sm, hi * 64 + i * 16 + r16);
#pragma unroll
            for (int j = 0; j < 5; ++j) acc[j][i] *= ez;
          }
        }
      }
      SCHED0;
      STAMP(6);
      if (wid == kq) {
#pragma unroll
        for (int i = 0; i < 4; ++i)
#pragma unroll
          for (int j = 0; j < 5; ++j) {
            u32x2 v = {pk2(acc[j][i][0], acc[j][i][1]), pk2(acc[j][i][2], acc[j][i][3])};
            *(__attribute__((address_space(3))) u32x2*)(sm + SPARE + i * 2560 + j * 512 + lane * 8) = v;
          }
      } else {
        const unsigned sbase = WS_SL + (((rt * 4 + ct) * 4 + wid) * 3 + ((kq - wid - 1) & 3)) * 10240;
#pragma unroll
        for (int i = 0; i < 4; ++i) {
          st16(pk8(acc[0][i], acc[1][i]), rws, l16 + i * 2560, sbase);
          st16(pk8(acc[2][i], acc[3][i]), rws, l16 + i * 2560 + 1024, sbase);
          u32x2 v = {pk2(acc[4][i][0], acc[4][i][1]), pk2(acc[4][i][2], acc[4][i][3])};
          st8(v, rws, lane * 8 + i * 2560 + 2048, sbase);
        }
      }
    }
    {
      f32x4 o[5] = {};
      bf16x8 ha[2], wa[10], hb[2], wb[10];
      const unsigned ho = l16 + wid * 1024, wo = l16 + 16384;
#define RD_O(H, W, SB, HF) do { _Pragma("unroll") for (int k = 0; k < 2; ++k) { H[k] = ldf((SB) + ho + (2 * (HF) + k) * 4096); \
      _Pragma("unroll") for (int j = 0; j < 5; ++j) W[k * 5 + j] = ldf((SB) + wo + ((2 * (HF) + k) * 5 + j) * 1024); } } while (0)
#define MM_O(H, W) do { _Pragma("unroll") for (int k = 0; k < 2; ++k) _Pragma("unroll") for (int j = 0; j < 5; ++j) o[j] = mma(W[k * 5 + j], H[k], o[j]); } while (0)
      u32x4 su[3], sv[3];
      u32x2 sw[3];
      const unsigned sl = WS_SL + ((rt * 4 + ct) * 4 + kq) * 30720 + wid * 2560, l8 = lane * 8;
      bool got = false;
#define LD_SLABS() do { _Pragma("unroll") for (int s = 0; s < 3; ++s) \
      asm volatile("buffer_load_dwordx4 %0, %3, %5, %6 offen sc1\n\tbuffer_load_dwordx4 %1, %3, %5, %6 offen offset:1024 sc1\n\t" \
                   "buffer_load_dwordx2 %2, %4, %5, %6 offen offset:2048 sc1" \
                   : "=&v"(su[s]), "=&v"(sv[s]), "=&v"(sw[s]) : "v"(l16), "v"(l8), "s"(rws), "s"(sl + s * 10240) : "memory"); } while (0)
      STAMP(7);
      RD_O(ha, wa, sb, 0);
#pragma unroll
      for (int n = 0; n < NT; ++n) {
        SCHED0;
        RD_O(hb, wb, sb, 1);
        MM_O(ha, wa);
        SCHED0;
        if (n == 1 && wid != kq) {
          waitv<0>();
          if (lane == 0)
            __hip_atomic_store((gu32*)(wsb + FL_SL + (rt * 4 + ct) * 128 + wid * 16 + ((kq - wid - 1) & 3) * 4), *(volatile lds_u32*)(sm + TOK_OFF),
                               __ATOMIC_RELAXED, __HIP_MEMORY_SCOPE_AGENT);
        }
        wait_lds(); bar(); DST(b35 + n);
        sb = nexts(sb);
        if (n + 1 < NT) RD_O(ha, wa, sb, 0);
        MM_O(hb, wb);
      }
      SCHED0;
      STAMP(8);
      wait_rdy<3>();
      LD_SLABS();
      (void)got;
      const float gi = __builtin_amdgcn_rcpf(1.0f + expz(sm, mm * 64 + wid * 16 + r16));
#pragma unroll
      for (int j = 0; j < 5; ++j) {
        const u32x2 v = *(const __attribute__((address_space(3))) u32x2*)(sm + SPARE + wid * 2560 + j * 512 + lane * 8);
        o[j] += (f32x4){bflo(v[0]), bfhi(v[0]), bflo(v[1]), bfhi(v[1])};
      }
      asm volatile("s_waitcnt vmcnt(0)" : "+v"(su[0]), "+v"(su[1]), "+v"(su[2]), "+v"(sv[0]), "+v"(sv[1]), "+v"(sv[2]), "+v"(sw[0]), "+v"(sw[1]), "+v"(sw[2])::"memory");
      STAMP(9);
#pragma unroll
      for (int s = 0; s < 3; ++s) {
        const u32x4 u = su[s], v = sv[s];
        const u32x2 w = sw[s];
        o[0] += (f32x4){bflo(u[0]), bfhi(u[0]), bflo(u[1]), bfhi(u[1])};
        o[1] += (f32x4){bflo(u[2]), bfhi(u[2]), bflo(u[3]), bfhi(u[3])};
        o[2] += (f32x4){bflo(v[0]), bfhi(v[0]), bflo(v[1]), bfhi(v[1])};
        o[3] += (f32x4){bflo(v[2]), bfhi(v[2]), bflo(v[3]), bfhi(v[3])};
        o[4] += (f32x4){bflo(w[0]), bfhi(w[0]), bflo(w[1]), bfhi(w[1])};
      }
      char* op = out + (size_t)(rt * 128 + mm * 64 + wid * 16 + r16) * (DM * 2) + (ct * 160 + odd * 80 + q * 4) * 2;
#pragma unroll
      for (int j = 0; j < 5; ++j) {
        u32x2 v = {pk2(o[j][0] * gi, o[j][1] * gi), pk2(o[j][2] * gi, o[j][3] * gi)};
        *(u32x2*)(op + j * 32) = v;
      }
      STAMP(10);
    }
  } else if (wid < 7) {
    if (wid == 4) loader<0>(x, mm ? ws_im : ws_re, mm ? wd_im : wd_re, wg, wu, rws, lane, rt, ct, kq);
    else if (wid == 5) loader<1>(x, mm ? ws_im : ws_re, mm ? wd_im : wd_re, wg, wu, rws, lane, rt, ct, kq);
    else loader<2>(x, mm ? ws_im : ws_re, mm ? wd_im : wd_re, wg, wu, rws, lane, rt, ct, kq);
  } else {
    volatile lds_u32* rdy = (volatile lds_u32*)(sm + RDY_OFF);
    if (lane == 0) { *rdy = 0; dmaw(rws, WS_FLAG, PK_EP); }
    if (odd) {
      const rsrc_t rg = mk_rsrc(gate_w, DM * 2);
      dma1(rg, lane * 16, 0, GW_OFF);
      dma1(rg, lane * 16, 1024, GW_OFF + 1024);
    }
    bar(); bar(); bar();
    const int unit = odd ? (rt < 8 ? 128 + (mm * 8 + rt) * 4 + ct : -1) : (mm * 16 + rt) * 4 + ct;
    const int ufb = unit >> 2, uct = unit & 3;
    if (unit >= 0) {
      const rsrc_t rdn = mk_rsrc(wdn, FF * DM * 2);
#pragma unroll 1
      for (int p = 0; p < 10; ++p) {
        const int c = p * 64 + lane, row = (c * 3277) >> 16;
        dma1(rdn, row * (DM * 2) + (c - row * 20) * 16, ufb * 32 * (DM * 2) + uct * 320, SPARE + p * 1024);
      }
    }
    bar(); bar(); bar(); bar();
    waitv<0>();
    const unsigned token = __builtin_amdgcn_readfirstlane(*(volatile lds_u32*)(sm + PK_EP)) + 0x9E3779B9u;
    *(volatile lds_u32*)(sm + PK_H + lane * 4) = ~token;
    if (lane < 24) *(volatile lds_u32*)(sm + PK_AB + lane * 4) = lane == 22 ? token : (lane == 20 ? 0u : ~token);
    if (unit >= 0) {
      const int g = lane >> 4, qq = (lane & 15) >> 2;
      const unsigned ro = SPARE + (4 * g + qq) * 320 + (lane & 3) * 8;
      const bool w1 = ufb < 8 * NS;
      const int f2 = ufb - 8 * NS;
      const unsigned wb = w1 ? WS_W1 + ((uct * 4 * NS + (ufb >> 1)) * 20 + (ufb & 1)) * 1024 : WS_W2 + ((uct * 2 * NT + (f2 >> 2)) * 20 + (f2 & 3) * 5) * 1024;
      const unsigned se = w1 ? 2048 : 1024, sh = w1 ? 10240 : NT * 20480;
#pragma unroll
      for (int e = 0; e < 10; ++e) {
        s16x4 lo = __builtin_amdgcn_ds_read_tr16_b64_v4i16((__attribute__((address_space(3))) s16x4*)(sm + ro + e * 32));
        s16x4 hi = __builtin_amdgcn_ds_read_tr16_b64_v4i16((__attribute__((address_space(3))) s16x4*)(sm + ro + e * 32 + 16 * 320));
        s16x8 v = __builtin_shufflevector(lo, hi, 0, 1, 2, 3, 4, 5, 6, 7);
        st16(__builtin_bit_cast(u32x4, v), rws, lane * 16, wb + (e / 5) * sh + (e % 5) * se);
      }
    }
    wait_lds();
    bar();
    const unsigned fa = FL_AB + (mm * 4 + ct) * 128 + (odd ? max(lane & 15, N3A >> 1) : min(lane & 15, (N3A - 1) >> 1)) * 4;
    const unsigned fh = lane < 16 ? FL_H + rt * 128 + lane * 4 : FL_WD + ct * 256 + (lane - 16) * 4;
    const unsigned fs = FL_SL + (rt * 4 + ct) * 128 + kq * 16 + min(lane & 3, 2) * 4;
#define PUBLISH(FOFF) do { if (lane == 0) __hip_atomic_store((gu32*)(wsb + (FOFF)), token, __ATOMIC_RELAXED, __HIP_MEMORY_SCOPE_AGENT); } while (0)
#define BARS(CNT) do { _Pragma("unroll 1") for (int c_ = (CNT); c_ > 0; --c_) bar(); } while (0)
#define GATE(CNT, L, FOFF, NL, LOFF, ACT) do { bool got_ = false; \
      _Pragma("unroll 1") for (int c_ = (CNT);; --c_) { \
        if (!got_) { \
          got_ = __all(*(volatile lds_u32*)(sm + (LOFF) + (lane & ((NL) - 1)) * 4) == token); \
          if (!got_ && c_ == 0) { \
            _Pragma("unroll 1") for (unsigned spin = 0; spin < SPIN_MAX; ++spin) { \
              if (__all(__hip_atomic_load((const gu32*)(wsb + (FOFF)), __ATOMIC_RELAXED, __HIP_MEMORY_SCOPE_AGENT) == token)) break; \
              __builtin_amdgcn_s_sleep(1); } \
            got_ = true; } \
          if (got_) { if (lane == 0) *rdy = (L); ACT; wait_lds(); } \
          else if (lane < (NL)) dmaw(rws, (FOFF), (LOFF)); } \
        if (c_ == 0) break; \
        bar(); } } while (0)
    if (odd) {
      BARS(5);
      PUBLISH(FL_AB + (mm * 4 + ct) * 128 + rt * 4);
      bar();
    }
    GATE(odd ? 4 : 3, 1, fa, 16, PK_AB, { waitv<0>(); if (unit >= 0) PUBLISH(FL_WD + uct * 256 + ufb * 4); });
    STAMP(1);
    BARS(odd ? 4 + HP : 2 + HP);
    PUBLISH(FL_H + rt * 128 + (ct * 4 + kq) * 4);
    bar();
    GATE(n3 - HP - 5, 2, fh, 64, PK_H, { if (!odd && lane < 32) dma1(rws, lane * 16, WS_Z + ((rt * 4 + ct) * 2 + mm) * 512, Z_OFF); });
    bar(); bar();
    waitv<0>();
    BARS(NS + 2);
    GATE(NT - 1, 3, fs, 4, PK_SL, {});
    STAMP(2);
    STAMP(3);
    if (bid == 0) {
#pragma unroll 1
      for (unsigned spin = 0; spin < SPIN_MAX; ++spin) {
        u32x4 v = __builtin_amdgcn_raw_buffer_load_b128(rws, FL_H + (lane >> 2) * 128 + (lane & 3) * 16, 0, 16);
        if (__all(v[0] == token && v[1] == token && v[2] == token && v[3] == token)) break;
        __builtin_amdgcn_s_sleep(1);
      }
      PUBLISH(WS_FLAG);
    }
    waitv<0>();
  }
}

extern "C" void kernel_launch(void* const* d_in, const int* in_sizes, int n_in, void* d_out, int out_size, void* d_ws, size_t ws_size,
                              hipStream_t stream) {
  (void)in_sizes; (void)n_in; (void)out_size; (void)ws_size;
  (void)hipFuncSetAttribute((const void*)fused_kernel, hipFuncAttributeMaxDynamicSharedMemorySize, LDS_BYTES);
  fused_kernel<<<256, 512, LDS_BYTES, stream>>>((const char*)d_in[0], (const char*)d_in[1], (const char*)d_in[2], (const char*)d_in[3],
                                                (const char*)d_in[4], (const char*)d_in[6], (const char*)d_in[7], (const char*)d_in[8],
                                                (const char*)d_in[9], (char*)d_out, (char*)d_ws);
}
```

```cpp
#include <hip/hip_runtime.h>
#include <hip/hip_bf16.h>
#include <stdint.h>

typedef __attribute__((ext_vector_type(8))) __bf16 bf16x8;
typedef __attribute__((ext_vector_type(2))) __bf16 bf16x2;
typedef __attribute__((ext_vector_type(4))) short s16x4;
typedef __attribute__((ext_vector_type(8))) short s16x8;
typedef __attribute__((ext_vector_type(4))) float f32x4;
typedef __attribute__((ext_vector_type(2))) float f32x2;
typedef __attribute__((ext_vector_type(4))) unsigned u32x4;
typedef __attribute__((ext_vector_type(2))) unsigned u32x2;
typedef __attribute__((address_space(3))) char* lds_p;
typedef __attribute__((address_space(1))) unsigned gu32;
typedef __attribute__((address_space(3))) unsigned lds_u32;
typedef __amdgpu_buffer_rsrc_t rsrc_t;

#define DEV __device__ __forceinline__

constexpr int SEQ = 2048, DM = 640, FF = 1536;
#define N3A 17
#define NS 4
constexpr int NT = 12 - 2 * NS;
constexpr int SLOT_A = 49152, SLOT_B = 36864;
constexpr int SPARE = 147456;
constexpr int GW_OFF = SPARE + 10240;
constexpr int Z_OFF = SPARE + 15360;
constexpr int PK_H = SPARE + 15872;
constexpr int PK_AB = SPARE + 16128, PK_SL = SPARE + 16192, RDY_OFF = SPARE + 16208, PK_EP = SPARE + 16212, TOK_OFF = SPARE + 16216;
constexpr int LDS_BYTES = 163840;
constexpr unsigned WS_FLAG = 0;
constexpr unsigned FL_AB = 128, FL_H = 2048, FL_WD = 4096, FL_SL = 8192;
constexpr unsigned WS_Z = 16384;
constexpr unsigned WS_AB = WS_Z + 65536;
constexpr unsigned WS_H1 = WS_AB + 2 * 4 * 32 * 20480;
constexpr unsigned WS_H2 = WS_H1 + 16 * 4 * NS * 16384;
constexpr unsigned WS_W1 = WS_H2 + 32 * NT * 16384;
constexpr unsigned WS_W2 = WS_W1 + 4 * 4 * NS * 20480;
constexpr unsigned WS_SL = WS_W2 + 8 * NT * 20480;
constexpr unsigned WS_END = WS_SL + 256 * 30720;
constexpr unsigned SPIN_MAX = 200000;
constexpr float LOG2E = 1.44269504088896f;

template <int N> DEV void waitv() { asm volatile("s_waitcnt vmcnt(%0)" ::"n"(N) : "memory"); }
DEV void bar() {
  asm volatile("" ::: "memory");
  __builtin_amdgcn_s_barrier();
  asm volatile("" ::: "memory");
}
DEV void wait_lds() { __builtin_amdgcn_s_waitcnt(0xc07f); }
#define SCHED0 __builtin_amdgcn_sched_barrier(0)

DEV void dma1(rsrc_t r, unsigned voff, unsigned soff, unsigned ldsb) {
  asm volatile("s_mov_b32 m0, %0\n\ts_nop 3\n\tbuffer_load_dwordx4 %1, %2, %3 offen sc1 lds" ::"s"(ldsb), "v"(voff), "s"(r), "s"(soff) : "memory");
}
#define SCX " sc1"
DEV void dma4x(rsrc_t r, const unsigned (&v)[4], unsigned soff, unsigned ldsb) {
  asm volatile("s_mov_b32 m0, %0\n\ts_nop 0\n\t"
               "buffer_load_dwordx4 %1, %5, %6 offen" SCX " lds\n\tbuffer_load_dwordx4 %2, %5, %6 offen offset:1024" SCX " lds\n\t"
               "buffer_load_dwordx4 %3, %5, %6 offen offset:2048" SCX " lds\n\tbuffer_load_dwordx4 %4, %5, %6 offen offset:3072" SCX " lds"
               ::"s"(ldsb), "v"(v[0]), "v"(v[1]), "v"(v[2]), "v"(v[3]), "s"(r), "s"(soff) : "memory");
}
DEV void dmaw(rsrc_t r, unsigned voff, unsigned ldsb) {
  asm volatile("s_mov_b32 m0, %0\n\ts_nop 3\n\tbuffer_load_dword %1, %2, 0 offen sc1 lds" ::"s"(ldsb), "v"(voff), "s"(r) : "memory");
}
DEV void warm(const char* p) {
  asm volatile("s_mov_b32 m0, %0\n\ts_nop 0\n\tglobal_load_lds_dword %1, off sc1" ::"s"(PK_H), "v"(p) : "memory");
}
DEV rsrc_t mk_rsrc(const void* p, unsigned bytes) { return __builtin_amdgcn_make_buffer_rsrc((void*)p, (short)0, (int)bytes, 0x00020000); }
DEV bf16x8 ldf(lds_p p) { return *(const __attribute__((address_space(3))) bf16x8*)p; }
DEV bf16x8 ldtr(lds_p p) {
  s16x4 lo = __builtin_amdgcn_ds_read_tr16_b64_v4i16((__attribute__((address_space(3))) s16x4*)p);
  s16x4 hi = __builtin_amdgcn_ds_read_tr16_b64_v4i16((__attribute__((address_space(3))) s16x4*)(p + 512));
  s16x8 v = __builtin_shufflevector(lo, hi, 0, 1, 2, 3, 4, 5, 6, 7);
  return __builtin_bit_cast(bf16x8, v);
}
DEV f32x4 mma(bf16x8 a, bf16x8 b, f32x4 c) { return __builtin_amdgcn_mfma_f32_16x16x32_bf16(a, b, c, 0, 0, 0); }
DEV unsigned pk2(float a, float b) { f32x2 v = {a, b}; return __builtin_bit_cast(unsigned, __builtin_convertvector(v, bf16x2)); }
DEV u32x4 pk8(f32x4 a, f32x4 b) { u32x4 r = {pk2(a[0], a[1]), pk2(a[2], a[3]), pk2(b[0], b[1]), pk2(b[2], b[3])}; return r; }
DEV float bflo(unsigned v) { return __builtin_bit_cast(float, v << 16); }
DEV float bfhi(unsigned v) { return __builtin_bit_cast(float, v & 0xffff0000u); }
DEV void st16(u32x4 v, rsrc_t r, unsigned off, unsigned soff) { __builtin_amdgcn_raw_buffer_store_b128(v, r, off, soff, 16); }
DEV void st8(u32x2 v, rsrc_t r, unsigned off, unsigned soff) { __builtin_amdgcn_raw_buffer_store_b64(v, r, off, soff, 16); }
template <int NM, int NR> DEV void interleave() {
#pragma unroll
  for (int i = 0; i < NR; ++i) {
    __builtin_amdgcn_sched_group_barrier(0x008, 1, 0);
    __builtin_amdgcn_sched_group_barrier(0x100, 1, 0);
  }
  if constexpr (NM > NR) __builtin_amdgcn_sched_group_barrier(0x008, NM - NR, 0);
}
#define STAMP(i) do {} while (0)
#define DST(i) do {} while (0)

DEV f32x4 glu4(f32x4 G, f32x4 U) {
  f32x4 r;
#pragma unroll
  for (int j = 0; j < 4; ++j) r[j] = U[j] * __builtin_amdgcn_rcpf(1.0f + __builtin_amdgcn_exp2f(G[j] * -LOG2E));
  return r;
}
DEV float expz(lds_p sm, int row) {
  const float z = *(const __attribute__((address_space(3))) float*)(sm + Z_OFF + row * 4);
  return __builtin_amdgcn_exp2f(fminf(fmaxf(z, -60.0f), 60.0f) * LOG2E);
}
DEV int rdy_now() {
  int s; unsigned v;
  asm volatile("ds_read_b32 %1, %2\n\ts_waitcnt lgkmcnt(0)\n\tv_readfirstlane_b32 %0, %1" : "=s"(s), "=&v"(v) : "v"(RDY_OFF) : "memory");
  return s;
}
template <int LVL> DEV void wait_rdy() {
  unsigned s, c, v;
  asm volatile("s_mov_b32 %1, %4\n"
               "1:\n\tds_read_b32 %2, %3\n\ts_waitcnt lgkmcnt(0)\n\tv_readfirstlane_b32 %0, %2\n\ts_cmp_ge_i32 %0, %5\n\ts_cbranch_scc1 2f\n\t"
               "s_sleep 1\n\ts_sub_u32 %1, %1, 1\n\ts_cbranch_scc0 1b\n2:"
               : "=&s"(s), "=&s"(c), "=&v"(v) : "v"(RDY_OFF), "n"(SPIN_MAX), "n"(LVL) : "memory", "scc");
}
DEV unsigned nexts(unsigned sb) { return sb == 3 * SLOT_B ? 0u : sb + SLOT_B; }
DEV unsigned nexta(unsigned sb) { return sb == 2 * SLOT_A ? 0u : sb + SLOT_A; }

template <int LW, unsigned GA, unsigned GB, bool FIVE = true>
DEV void issue(rsrc_t ra, const unsigned (&va)[4], unsigned sa, rsrc_t rb, const unsigned (&vb)[4], unsigned sc, unsigned sb) {
  if constexpr (LW == 0) {
#pragma unroll
    for (int g = 0; g < 3; ++g) dma4x(ra, va, sa + g * GA, sb + g * 4096);
  } else if constexpr (LW == 1) {
    dma4x(ra, va, sa + 3 * GA, sb + 3 * 4096);
#pragma unroll
    for (int g = 0; g < 2; ++g) dma4x(rb, vb, sc + g * GB, sb + 16384 + g * 4096);
  } else {
#pragma unroll
    for (int g = 2; g < (FIVE ? 5 : 4); ++g) dma4x(rb, vb, sc + g * GB, sb + 16384 + g * 4096);
  }
}
template <int LW> DEV void loader(const char* x, const char* wsq, const char* wd, const char* wg, const char* wu, rsrc_t rws,
                                  int lane, int rt, int ct, int kq) {
  const int odd = kq & 1, mm = kq >> 1, fg = 4 * mm + ct;
  const int lr = lane >> 3, lp = lane & 7;
  const unsigned chT = (lp ^ (lr & 6)) * 16;
  unsigned sb = 0;
  {
    unsigned vx[4], vw[4], vp[4];
#pragma unroll
    for (int j = 0; j < 4; ++j) {
      vx[j] = (lr + 8 * j) * (DM * 2) + chT - j * 1024;
      vw[j] = (lr + 8 * j) * (FF * 2) + (((lp >> 1) ^ (((lr >> 1) & 1) | ((j & 1) << 1))) * 2 + (lp & 1)) * 16 - j * 1024;
      vp[j] = (8 * (lane >> 5) + (lr & 3) + 16 * (j & 1) + 4 * (j >> 1)) * (DM * 2) + chT - j * 1024;
    }
    const rsrc_t rx = mk_rsrc(x, SEQ * DM * 2);
    const rsrc_t rw = mk_rsrc(LW == 2 ? wu : wg, DM * FF * 2);
    if (!odd) {
      unsigned so = LW == 0 ? rt * 128 * (DM * 2) : fg * 256;
      sb = 2 * SLOT_A;
#pragma unroll 1
      for (int j = 0; j < 10; ++j) {
        if (j == 3) { waitv<32>(); bar(); }
        if (j >= 3) { waitv<16>(); bar(); }
        if constexpr (LW == 0) {
#pragma unroll
          for (int g = 0; g < 4; ++g) dma4x(rx, vx, so + g * 32 * (DM * 2), sb + g * 4096);
          so += 128;
        } else {
#pragma unroll
          for (int g = 0; g < 4; ++g) dma4x(rw, vw, so + (g & 1) * 32 * (FF * 2) + (g >> 1) * 128, sb + LW * 16384 + g * 4096);
          so += 64 * (FF * 2);
        }
        sb = nexta(sb);
      }
      waitv<16>(); bar();
      sb = 0;
    } else {
      const rsrc_t rd = mk_rsrc(wd, DM * DM * 2);
      unsigned sa = rt * 128 * (DM * 2), sc = ct * 160 * (DM * 2);
#pragma unroll 1
      for (int j = 0; j < 10; ++j) {
        if (j == 4) { waitv<36>(); bar(); }
        if (j >= 4) { waitv<24>(); bar(); }
        issue<LW, 32 * (DM * 2), 32 * (DM * 2)>(rx, vp, sa, rd, vx, sc, sb);
        sa += 128; sc += 128; sb = nexts(sb);
      }
      sa = rt * 128 * (DM * 2); sc = 2048 + fg * 128 - (LW == 2 ? 64 * (FF * 2) : 0);
#pragma unroll 1
      for (int j = 10; j < 20; ++j) {
        waitv<LW == 2 ? 16 : 24>();
        bar();
        issue<LW, 32 * (DM * 2), 32 * (FF * 2), false>(rx, vx, sa, rw, vw, sc, sb);
        sa += 128; sc += 64 * (FF * 2); sb = nexts(sb);
      }
    }
  }
  const int n3 = odd ? 32 - N3A : N3A, s30 = odd ? N3A : 0;
  const unsigned vl[4] = {lane * 16u, lane * 16u, lane * 16u, lane * 16u};
  {
    unsigned vs[4];
#pragma unroll
    for (int k = 0; k < 4; ++k) vs[k] = (lr + 8 * k) * (SEQ * 2) + chT - k * 1024;
    const rsrc_t rq = mk_rsrc(wsq, SEQ * SEQ * 2);
    unsigned sa = rt * 128 * (SEQ * 2) + s30 * 128, sc = WS_AB + ((mm * 4 + ct) * 32 + s30) * 20480;
    int pb = odd ? 0 : 2;
#pragma unroll 1
    for (int j = 0; j < n3; ++j) {
      if (odd) {
        if (LW == 2 && j < 2) waitv<16>(); else waitv<24>();
        bar();
      } else if (j >= 4) {
        if (j == 4) { waitv<36>(); bar(); }
        waitv<24>(); bar();
      } else {
        while (pb > 0 && (j >= 3 - pb || rdy_now() < 1)) {
          if (pb == 2) { if (j > 0) waitv<12>(); else waitv<0>(); }
          bar(); --pb;
        }
      }
      if (j == 0) wait_rdy<1>();
      issue<LW, 32 * (SEQ * 2), 4096>(rq, vs, sa, rws, vl, sc, sb);
      sa += 128; sc += 20480; sb = nexts(sb);
    }
  }
  {
    unsigned sa = WS_H1 + (rt * 4 + kq) * NS * 16384, sc = WS_W1 + (ct * 4 + kq) * NS * 20480;
#pragma unroll 1
    for (int j = 0; j < NS + NT; ++j) {
      waitv<24>(); bar();
      if (j == 0) wait_rdy<2>();
      if (j == NS) { sa = WS_H2 + (rt * 2 + mm) * NT * 16384; sc = WS_W2 + (ct * 2 + odd) * NT * 20480; }
      issue<LW, 4096, 4096>(rws, vl, sa, rws, vl, sc, sb);
      sa += 16384; sc += 20480; sb = nexts(sb);
    }
  }
  waitv<24>(); bar();
  waitv<12>(); bar();
  waitv<0>(); bar();
  bar();
}

__global__ __launch_bounds__(512, 2) void fused_kernel(const char* __restrict__ x, const char* __restrict__ ws_re, const char* __restrict__ ws_im,
                                                       const char* __restrict__ wd_re, const char* __restrict__ wd_im,
                                                       const char* __restrict__ wg, const char* __restrict__ wu, const char* __restrict__ wdn,
                                                       const char* __restrict__ gate_w, char* __restrict__ out, char* wsb) {
  extern __shared__ __attribute__((aligned(16))) char smem_raw[];
  const lds_p sm = (lds_p)smem_raw;
  const int wid = __builtin_amdgcn_readfirstlane(threadIdx.x >> 6), lane = threadIdx.x & 63;
  const int bid = blockIdx.x, xcd = bid & 7, idx = bid >> 3;
  const int kq = xcd >> 1, rt = (xcd & 1) * 8 + (idx >> 2), ct = idx & 3;
  const int odd = kq & 1, mm = kq >> 1, fg = 4 * mm + ct;
  const int b2 = odd ? 20 : 10, n3 = odd ? 32 - N3A : N3A, s30 = odd ? N3A : 0;
  const int b3 = b2 + n3, b35 = b3 + NS, b4 = b35 + NT;
  constexpr int HP = 4;
  const rsrc_t rws = mk_rsrc(wsb, WS_END);
  STAMP(0);

  if (wid < 4) {
    const int r16 = lane & 15, q = lane >> 4;
    const unsigned t0 = r16 * 128 + ((q ^ (r16 & 6)) << 4), t1 = t0 ^ 64;
    const int q4 = r16 >> 2, fx = ((q4 >> 1) & 1) | ((q & 1) << 1);
    const unsigned xrow = (8 * q + q4) * 128 + (lane & 3) * 8;
    const int hi = wid >> 1, lo = wid & 1;
    const unsigned l16 = lane * 16;
    if (!odd) {
      f32x4 acc[8][4] = {};
      bf16x8 xa[4], wa[8], xb[4], wb[8];
      unsigned xo[4];
#pragma unroll
      for (int m = 0; m < 4; ++m) xo[m] = xrow + ((m ^ fx) << 5) + 16384 + lo * 8192;
      const unsigned ta = t0 + hi * 8192, tb = t1 + hi * 8192;
#define RD_GUW(X, W, SB, TO, KS) do { _Pragma("unroll") for (int i = 0; i < 4; ++i) X[i] = ldf(sm + (SB) + (TO) + i * 2048); \
      _Pragma("unroll") for (int m = 0; m < 4; ++m) { W[m] = ldtr(sm + (SB) + xo[m] + (KS) * 4096); W[4 + m] = ldtr(sm + (SB) + xo[m] + 16384 + (KS) * 4096); } } while (0)
#define MM_GUW(X, W) do { _Pragma("unroll") for (int j = 0; j < 8; ++j) _Pragma("unroll") for (int i = 0; i < 4; ++i) acc[j][i] = mma(W[j], X[i], acc[j][i]); } while (0)
      bar();
      STAMP(1);
      unsigned sb = 2 * SLOT_A;
      RD_GUW(xa, wa, sb, ta, 0);
#pragma unroll 1
      for (int n = 0; n < 10; ++n) {
        SCHED0;
        RD_GUW(xb, wb, sb, tb, 1);
        MM_GUW(xa, wa);
        interleave<32, 20>();
        SCHED0;
        wait_lds(); bar(); DST(n);
        sb = nexta(sb);
        RD_GUW(xa, wa, sb, ta, 0);
        MM_GUW(xb, wb);
        interleave<32, 20>();
      }
      SCHED0;
      STAMP(2);
      const bool h1 = fg < 2 * NS;
      const unsigned hbase = h1 ? WS_H1 + ((rt * 4 * NS + fg * 2 + lo) * 16 + hi * 8) * 1024
                                : WS_H2 + (((rt * 2 + hi) * NT + fg - 2 * NS) * 16 + lo * 8) * 1024;
      const unsigned sp = h1 ? 1024 : 4096, si = h1 ? 2048 : 1024;
#pragma unroll
      for (int i = 0; i < 4; ++i)
#pragma unroll
        for (int p = 0; p < 2; ++p)
          st16(pk8(glu4(acc[2 * p][i], acc[4 + 2 * p][i]), glu4(acc[2 * p + 1][i], acc[5 + 2 * p][i])), rws, l16, hbase + p * sp + i * si);
    } else {
      unsigned sb = 0;
      {
        f32x4 acc[4][5] = {};
        bf16x8 xa[4], wa[5], xb[4], wb[5];
        const unsigned ta = t0 + hi * 8192, tb = t1 + hi * 8192, ua = t0 + 16384 + lo * 10240, ub = t1 + 16384 + lo * 10240;
#define RD_TT(X, W, SB, TO, UO) do { _Pragma("unroll") for (int i = 0; i < 4; ++i) X[i] = ldf(sm + (SB) + (TO) + i * 2048); \
        _Pragma("unroll") for (int j = 0; j < 5; ++j) W[j] = ldf(sm + (SB) + (UO) + j * 2048); } while (0)
#define MM_AB(X, W) do { _Pragma("unroll") for (int j = 0; j < 5; ++j) _Pragma("unroll") for (int i = 0; i < 4; ++i) acc[i][j] = mma(X[i], W[j], acc[i][j]); } while (0)
        bar();
        STAMP(1);
        RD_TT(xa, wa, 0, ta, ua);
#pragma unroll 1
        for (int n = 0; n < 10; ++n) {
          SCHED0;
          RD_TT(xb, wb, sb, tb, ub);
          MM_AB(xa, wa);
          interleave<20, 9>();
          SCHED0;
          wait_lds(); bar(); DST(n);
          sb = nexts(sb);
          RD_TT(xa, wa, sb, ta, ua);
          MM_AB(xb, wb);
          interleave<20, 9>();
        }
        SCHED0;
        const unsigned abase = WS_AB + ((mm * 4 + ct) * 32 + rt * 2 + hi) * 20480 + lo * 10240;
        const unsigned sg = mm ? 0x80008000u : 0u;
#pragma unroll
        for (int j = 0; j < 5; ++j)
#pragma unroll
          for (int p = 0; p < 2; ++p) {
            u32x4 v = pk8(acc[2 * p][j], acc[2 * p + 1][j]);
            v ^= sg;
            st16(v, rws, l16 + j * 2048 + p * 1024, abase);
          }
      }
      STAMP(2);
      {
        f32x4 acc[4][4] = {}, zc[2] = {};
        bf16x8 xa[4], wa[5], xb[4], wb[5];
        unsigned xo[2];
#pragma unroll
        for (int m = 0; m < 2; ++m) xo[m] = xrow + (((2 * lo + m) ^ fx) << 5) + 16384;
        const unsigned ta = t0 + hi * 8192 + lo * 4096, tb = ta ^ 64, tc = ta ^ 4096, td = tc ^ 64;
        unsigned go = GW_OFF + q * 16;
#define RD_GUN(X, W, SB, TO, TP, KS) do { X[0] = ldf(sm + (SB) + (TO)); X[1] = ldf(sm + (SB) + (TO) + 2048); \
        X[2] = ldf(sm + (SB) + (TP)); X[3] = ldf(sm + (SB) + (TP) + 2048); W[4] = ldf(sm + go + (KS) * 64); \
        _Pragma("unroll") for (int m = 0; m < 2; ++m) { W[m] = ldtr(sm + (SB) + xo[m] + (KS) * 4096); W[2 + m] = ldtr(sm + (SB) + xo[m] + 8192 + (KS) * 4096); } } while (0)
#define MM_GUN(X, W) do { _Pragma("unroll") for (int j = 0; j < 4; ++j) _Pragma("unroll") for (int i = 0; i < 4; ++i) acc[j][i] = mma(W[j], X[i], acc[j][i]); \
        zc[0] = mma(W[4], X[0], zc[0]); zc[1] = mma(W[4], X[1], zc[1]); } while (0)
        RD_GUN(xa, wa, sb, ta, tc, 0);
#pragma unroll 1
        for (int n = 10; n < 20; ++n) {
          SCHED0;
          RD_GUN(xb, wb, sb, tb, td, 1);
          MM_GUN(xa, wa);
          interleave<18, 13>();
          SCHED0;
          if (n == 11) waitv<0>();
          wait_lds(); bar(); DST(n);
          sb = nexts(sb); go += 128;
          RD_GUN(xa, wa, sb, ta, tc, 0);
          MM_GUN(xb, wb);
          interleave<18, 13>();
        }
        SCHED0;
        if (q == 0) {
          const unsigned zr = (hi * 64 + lo * 32 + r16) * 4;
#pragma unroll
          for (int u = 0; u < 2; ++u) {
            *(__attribute__((address_space(3))) float*)(sm + Z_OFF + zr + u * 64) = zc[u][0];
            __builtin_amdgcn_raw_buffer_store_b32(__builtin_bit_cast(unsigned, zc[u][0]), rws, zr + u * 64, WS_Z + ((rt * 4 + ct) * 2 + mm) * 512, 16);
          }
        }
        const int fb = 32 + fg * 2 + lo - 8 * NS;
        const unsigned hbase = WS_H2 + (((rt * 2 + hi) * NT + (fb >> 2)) * 16 + (fb & 3) * 4) * 1024;
#pragma unroll
        for (int i = 0; i < 4; ++i)
          st16(pk8(glu4(acc[0][i], acc[2][i]), glu4(acc[1][i], acc[3][i])), rws, l16 + (i & 1) * 1024, hbase + ((i >> 1) ^ lo) * 2048);
      }
    }
    STAMP(3);
    unsigned sb = 0;
    {
      f32x4 acc[5][4] = {};
      bf16x8 xa[4], wa[5], xb[4], wb[5];
      unsigned ta = t0 + hi * 8192, tb = t1 + hi * 8192;
      const unsigned ua = l16 + 16384 + lo * 10240, ub = ua + 1024;
#define MM_Y(X, W) do { _Pragma("unroll") for (int j = 0; j < 5; ++j) _Pragma("unroll") for (int i = 0; i < 4; ++i) acc[j][i] = mma(W[j], X[i], acc[j][i]); } while (0)
      if (!odd) bar();
      STAMP(4);
      RD_TT(xa, wa, 0, ta, ua);
#pragma unroll 1
      for (int n = b2; n < b35; ++n) {
        if (n == b3) {
          STAMP(5);
#pragma unroll
          for (int i = 0; i < 4; ++i) {
            const float ez = expz(sm, hi * 64 + i * 16 + r16);
#pragma unroll
            for (int j = 0; j < 5; ++j) acc[j][i] *= ez;
          }
          tb = l16 + hi * 8192 + 1024;
        }
        SCHED0;
        RD_TT(xb, wb, sb, tb, ub);
        MM_Y(xa, wa);
        interleave<20, 9>();
        SCHED0;
        if (n == b2 + HP) waitv<0>();
        if (n == b3 - 1) ta = l16 + hi * 8192;
        wait_lds(); bar(); DST(n);
        sb = nexts(sb);
        RD_TT(xa, wa, sb, ta, ua);
        MM_Y(xb, wb);
        interleave<20, 9>();
      }
      SCHED0;
      STAMP(6);
      if (wid == kq) {
#pragma unroll
        for (int i = 0; i < 4; ++i)
#pragma unroll
          for (int j = 0; j < 5; ++j) {
            u32x2 v = {pk2(acc[j][i][0], acc[j][i][1]), pk2(acc[j][i][2], acc[j][i][3])};
            *(__attribute__((address_space(3))) u32x2*)(sm + SPARE + i * 2560 + j * 512 + lane * 8) = v;
          }
      } else {
        const unsigned sbase = WS_SL + (((rt * 4 + ct) * 4 + wid) * 3 + ((kq - wid - 1) & 3)) * 10240;
#pragma unroll
        for (int i = 0; i < 4; ++i) {
          st16(pk8(acc[0][i], acc[1][i]), rws, l16 + i * 2560, sbase);
          st16(pk8(acc[2][i], acc[3][i]), rws, l16 + i * 2560 + 1024, sbase);
          u32x2 v = {pk2(acc[4][i][0], acc[4][i][1]), pk2(acc[4][i][2], acc[4][i][3])};
          st8(v, rws, lane * 8 + i * 2560 + 2048, sbase);
        }
      }
    }
    {
      f32x4 o[5] = {};
      bf16x8 ha[2], wa[10], hb[2], wb[10];
      const unsigned ho = l16 + wid * 1024, wo = l16 + 16384;
#define RD_O(H, W, SB, HF) do { _Pragma("unroll") for (int k = 0; k < 2; ++k) { H[k] = ldf(sm + (SB) + ho + (2 * (HF) + k) * 4096); \
      _Pragma("unroll") for (int j = 0; j < 5; ++j) W[k * 5 + j] = ldf(sm + (SB) + wo + ((2 * (HF) + k) * 5 + j) * 1024); } } while (0)
#define MM_O(H, W) do { _Pragma("unroll") for (int k = 0; k < 2; ++k) _Pragma("unroll") for (int j = 0; j < 5; ++j) o[j] = mma(W[k * 5 + j], H[k], o[j]); } while (0)
      u32x4 su[3], sv[3];
      u32x2 sw[3];
      const unsigned sl = WS_SL + ((rt * 4 + ct) * 4 + kq) * 30720 + wid * 2560, l8 = lane * 8;
      bool got = false;
#define LD_SLABS() do { _Pragma("unroll") for (int s = 0; s < 3; ++s) \
      asm volatile("buffer_load_dwordx4 %0, %3, %5, %6 offen sc1\n\tbuffer_load_dwordx4 %1, %3, %5, %6 offen offset:1024 sc1\n\t" \
                   "buffer_load_dwordx2 %2, %4, %5, %6 offen offset:2048 sc1" \
                   : "=&v"(su[s]), "=&v"(sv[s]), "=&v"(sw[s]) : "v"(l16), "v"(l8), "s"(rws), "s"(sl + s * 10240) : "memory"); } while (0)
      STAMP(7);
      RD_O(ha, wa, sb, 0);
#pragma unroll
      for (int n = 0; n < NT; ++n) {
        SCHED0;
        RD_O(hb, wb, sb, 1);
        MM_O(ha, wa);
        SCHED0;
        if (n == 1 && wid != kq) {
          waitv<0>();
          if (lane == 0)
            __hip_atomic_store((gu32*)(wsb + FL_SL + (rt * 4 + ct) * 128 + wid * 16 + ((kq - wid - 1) & 3) * 4), *(volatile lds_u32*)(sm + TOK_OFF),
                               __ATOMIC_RELAXED, __HIP_MEMORY_SCOPE_AGENT);
        }
        wait_lds(); bar(); DST(b35 + n);
        sb = nexts(sb);
        if (n + 1 < NT) RD_O(ha, wa, sb, 0);
        MM_O(hb, wb);
      }
      SCHED0;
      STAMP(8);
      wait_rdy<3>();
      LD_SLABS();
      (void)got;
      const float gi = __builtin_amdgcn_rcpf(1.0f + expz(sm, mm * 64 + wid * 16 + r16));
#pragma unroll
      for (int j = 0; j < 5; ++j) {
        const u32x2 v = *(const __attribute__((address_space(3))) u32x2*)(sm + SPARE + wid * 2560 + j * 512 + lane * 8);
        o[j] += (f32x4){bflo(v[0]), bfhi(v[0]), bflo(v[1]), bfhi(v[1])};
      }
      asm volatile("s_waitcnt vmcnt(0)" : "+v"(su[0]), "+v"(su[1]), "+v"(su[2]), "+v"(sv[0]), "+v"(sv[1]), "+v"(sv[2]), "+v"(sw[0]), "+v"(sw[1]), "+v"(sw[2])::"memory");
      STAMP(9);
#pragma unroll
      for (int s = 0; s < 3; ++s) {
        const u32x4 u = su[s], v = sv[s];
        const u32x2 w = sw[s];
        o[0] += (f32x4){bflo(u[0]), bfhi(u[0]), bflo(u[1]), bfhi(u[1])};
        o[1] += (f32x4){bflo(u[2]), bfhi(u[2]), bflo(u[3]), bfhi(u[3])};
        o[2] += (f32x4){bflo(v[0]), bfhi(v[0]), bflo(v[1]), bfhi(v[1])};
        o[3] += (f32x4){bflo(v[2]), bfhi(v[2]), bflo(v[3]), bfhi(v[3])};
        o[4] += (f32x4){bflo(w[0]), bfhi(w[0]), bflo(w[1]), bfhi(w[1])};
      }
      char* op = out + (size_t)(rt * 128 + mm * 64 + wid * 16 + r16) * (DM * 2) + (ct * 160 + odd * 80 + q * 4) * 2;
#pragma unroll
      for (int j = 0; j < 5; ++j) {
        u32x2 v = {pk2(o[j][0] * gi, o[j][1] * gi), pk2(o[j][2] * gi, o[j][3] * gi)};
        *(u32x2*)(op + j * 32) = v;
      }
      STAMP(10);
    }
  } else if (wid < 7) {
    if (wid == 4) loader<0>(x, mm ? ws_im : ws_re, mm ? wd_im : wd_re, wg, wu, rws, lane, rt, ct, kq);
    else if (wid == 5) loader<1>(x, mm ? ws_im : ws_re, mm ? wd_im : wd_re, wg, wu, rws, lane, rt, ct, kq);
    else loader<2>(x, mm ? ws_im : ws_re, mm ? wd_im : wd_re, wg, wu, rws, lane, rt, ct, kq);
  } else {
    volatile lds_u32* rdy = (volatile lds_u32*)(sm + RDY_OFF);
    if (lane == 0) { *rdy = 0; dmaw(rws, WS_FLAG, PK_EP); }
    if (odd) {
      const rsrc_t rg = mk_rsrc(gate_w, DM * 2);
      dma1(rg, lane * 16, 0, GW_OFF);
      dma1(rg, lane * 16, 1024, GW_OFF + 1024);
    }
    bar(); bar(); bar();
    const int unit = odd ? (rt < 8 ? 128 + (mm * 8 + rt) * 4 + ct : -1) : (mm * 16 + rt) * 4 + ct;
    const int ufb = unit >> 2, uct = unit & 3;
    if (unit >= 0) {
      const rsrc_t rdn = mk_rsrc(wdn, FF * DM * 2);
#pragma unroll 1
      for (int p = 0; p < 10; ++p) {
        const int c = p * 64 + lane, row = (c * 3277) >> 16;
        dma1(rdn, row * (DM * 2) + (c - row * 20) * 16, ufb * 32 * (DM * 2) + uct * 320, SPARE + p * 1024);
      }
    }
    bar(); bar(); bar(); bar();
    waitv<0>();
    const unsigned token = __builtin_amdgcn_readfirstlane(*(volatile lds_u32*)(sm + PK_EP)) + 0x9E3779B9u;
    *(volatile lds_u32*)(sm + PK_H + lane * 4) = ~token;
    if (lane < 24) *(volatile lds_u32*)(sm + PK_AB + lane * 4) = lane == 22 ? token : (lane == 20 ? 0u : ~token);
    if (unit >= 0) {
      const int g = lane >> 4, qq = (lane & 15) >> 2;
      const unsigned ro = SPARE + (4 * g + qq) * 320 + (lane & 3) * 8;
      const bool w1 = ufb < 8 * NS;
      const int f2 = ufb - 8 * NS;
      const unsigned wb = w1 ? WS_W1 + ((uct * 4 * NS + (ufb >> 1)) * 20 + (ufb & 1)) * 1024 : WS_W2 + ((uct * 2 * NT + (f2 >> 2)) * 20 + (f2 & 3) * 5) * 1024;
      const unsigned se = w1 ? 2048 : 1024, sh = w1 ? 10240 : NT * 20480;
#pragma unroll
      for (int e = 0; e < 10; ++e) {
        s16x4 lo = __builtin_amdgcn_ds_read_tr16_b64_v4i16((__attribute__((address_space(3))) s16x4*)(sm + ro + e * 32));
        s16x4 hi = __builtin_amdgcn_ds_read_tr16_b64_v4i16((__attribute__((address_space(3))) s16x4*)(sm + ro + e * 32 + 16 * 320));
        s16x8 v = __builtin_shufflevector(lo, hi, 0, 1, 2, 3, 4, 5, 6, 7);
        st16(__builtin_bit_cast(u32x4, v), rws, lane * 16, wb + (e / 5) * sh + (e % 5) * se);
      }
    }
    wait_lds();
    bar();
    const unsigned fa = FL_AB + (mm * 4 + ct) * 128 + (odd ? max(lane & 15, N3A >> 1) : min(lane & 15, (N3A - 1) >> 1)) * 4;
    const unsigned fh = lane < 16 ? FL_H + rt * 128 + lane * 4 : FL_WD + ct * 256 + (lane - 16) * 4;
    const unsigned fs = FL_SL + (rt * 4 + ct) * 128 + kq * 16 + min(lane & 3, 2) * 4;
#define PUBLISH(FOFF) do { if (lane == 0) __hip_atomic_store((gu32*)(wsb + (FOFF)), token, __ATOMIC_RELAXED, __HIP_MEMORY_SCOPE_AGENT); } while (0)
#define BARS(CNT) do { _Pragma("unroll 1") for (int c_ = (CNT); c_ > 0; --c_) bar(); } while (0)
#define GATE(CNT, L, FOFF, NL, LOFF, ACT) do { bool got_ = false; \
      _Pragma("unroll 1") for (int c_ = (CNT);; --c_) { \
        if (!got_) { \
          got_ = __all(*(volatile lds_u32*)(sm + (LOFF) + (lane & ((NL) - 1)) * 4) == token); \
          if (!got_ && c_ == 0) { \
            _Pragma("unroll 1") for (unsigned spin = 0; spin < SPIN_MAX; ++spin) { \
              if (__all(__hip_atomic_load((const gu32*)(wsb + (FOFF)), __ATOMIC_RELAXED, __HIP_MEMORY_SCOPE_AGENT) == token)) break; \
              __builtin_amdgcn_s_sleep(1); } \
            got_ = true; } \
          if (got_) { if (lane == 0) *rdy = (L); ACT; wait_lds(); } \
          else if (lane < (NL)) dmaw(rws, (FOFF), (LOFF)); } \
        if (c_ == 0) break; \
        bar(); } } while (0)
    if (odd) {
      BARS(5);
      PUBLISH(FL_AB + (mm * 4 + ct) * 128 + rt * 4);
      bar();
    }
    GATE(odd ? 4 : 3, 1, fa, 16, PK_AB, { waitv<0>(); if (unit >= 0) PUBLISH(FL_WD + uct * 256 + ufb * 4); });
    STAMP(1);
    BARS(odd ? 4 + HP : 2 + HP);
    PUBLISH(FL_H + rt * 128 + (ct * 4 + kq) * 4);
    bar();
    GATE(n3 - HP - 5, 2, fh, 64, PK_H, { if (!odd && lane < 32) dma1(rws, lane * 16, WS_Z + ((rt * 4 + ct) * 2 + mm) * 512, Z_OFF); });
    bar(); bar();
    waitv<0>();
    BARS(NS + 2);
    GATE(NT - 1, 3, fs, 4, PK_SL, {});
    STAMP(2);
    STAMP(3);
    if (bid == 0) {
#pragma unroll 1
      for (unsigned spin = 0; spin < SPIN_MAX; ++spin) {
        u32x4 v = __builtin_amdgcn_raw_buffer_load_b128(rws, FL_H + (lane >> 2) * 128 + (lane & 3) * 16, 0, 16);
        if (__all(v[0] == token && v[1] == token && v[2] == token && v[3] == token)) break;
        __builtin_amdgcn_s_sleep(1);
      }
      PUBLISH(WS_FLAG);
    }
    waitv<0>();
  }
}

extern "C" void kernel_launch(void* const* d_in, const int* in_sizes, int n_in, void* d_out, int out_size, void* d_ws, size_t ws_size,
                              hipStream_t stream) {
  (void)in_sizes; (void)n_in; (void)out_size; (void)ws_size;
  (void)hipFuncSetAttribute((const void*)fused_kernel, hipFuncAttributeMaxDynamicSharedMemorySize, LDS_BYTES);
  fused_kernel<<<256, 512, LDS_BYTES, stream>>>((const char*)d_in[0], (const char*)d_in[1], (const char*)d_in[2], (const char*)d_in[3],
                                                (const char*)d_in[4], (const char*)d_in[6], (const char*)d_in[7], (const char*)d_in[8],
                                                (const char*)d_in[9], (char*)d_out, (char*)d_ws);
}
```

```cpp
#include <hip/hip_runtime.h>
#include <hip/hip_bf16.h>
#include <stdint.h>

typedef __attribute__((ext_vector_type(8))) __bf16 bf16x8;
typedef __attribute__((ext_vector_type(2))) __bf16 bf16x2;
typedef __attribute__((ext_vector_type(4))) short s16x4;
typedef __attribute__((ext_vector_type(8))) short s16x8;
typedef __attribute__((ext_vector_type(4))) float f32x4;
typedef __attribute__((ext_vector_type(2))) float f32x2;
typedef __attribute__((ext_vector_type(4))) unsigned u32x4;
typedef __attribute__((ext_vector_type(2))) unsigned u32x2;
typedef __attribute__((ext_vector_type(16))) unsigned u32x16;
typedef __attribute__((address_space(1))) char* gptr;
typedef __attribute__((address_space(3))) char* lds_p;
typedef __attribute__((address_space(1))) unsigned gu32;
typedef __attribute__((address_space(3))) unsigned lds_u32;
typedef __amdgpu_buffer_rsrc_t rsrc_t;

#define DEV __device__ __forceinline__

constexpr int SEQ = 2048, DM = 640, FF = 1536;
#define N3A 19
#define NS 4
constexpr int NT = 12 - 2 * NS;
constexpr int SLOT_A = 49152, SLOT_B = 36864;
constexpr int SPARE = 147456;
constexpr int GW_OFF = SPARE + 10240;
constexpr int PAD_OFF = SPARE + 12288;
constexpr int Z_OFF = SPARE + 15360;
constexpr int PK_H = SPARE + 15872;
constexpr int PK_AB = SPARE + 13312, PK_A = SPARE + 16128, PK_SL = SPARE + 16192, RDY_OFF = SPARE + 16208, PK_EP = SPARE + 16212, TOK_OFF = SPARE + 16216;
constexpr int LDS_BYTES = 163840;
constexpr unsigned WS_FLAG = 0;
constexpr unsigned FL_H = 2048, FL_WD = 4096, FL_SL = 8192, FL_AB = 16384;
constexpr unsigned WS_AB = 32768;
constexpr unsigned WS_H1 = WS_AB + 2 * 4 * 32 * 20480;
constexpr unsigned WS_H2 = WS_H1 + 16 * 4 * NS * 16384;
constexpr unsigned WS_W1 = WS_H2 + 32 * NT * 16384;
constexpr unsigned WS_W2 = WS_W1 + 4 * 4 * NS * 20480;
constexpr unsigned WS_SL = WS_W2 + 8 * NT * 20480;
constexpr unsigned WS_END = WS_SL + 256 * 30720;
constexpr unsigned SPIN_MAX = 200000;
#define CODEB 28300
constexpr unsigned CODE_BYTES = CODEB;
constexpr float LOG2E = 1.44269504088896f;

template <int N> DEV void waitv() { asm volatile("s_waitcnt vmcnt(%0)" ::"n"(N) : "memory"); }
DEV void bar() {
  asm volatile("" ::: "memory");
  __builtin_amdgcn_s_barrier();
  asm volatile("" ::: "memory");
}
DEV void wait_lds() { __builtin_amdgcn_s_waitcnt(0xc07f); }
#define SCHED0 __builtin_amdgcn_sched_barrier(0)
#define ALN(K) do {} while (0)
#define KAL 0

DEV void dma1(rsrc_t r, unsigned voff, unsigned soff, unsigned ldsb) {
  asm volatile("s_mov_b32 m0, %0\n\ts_nop 3\n\tbuffer_load_dwordx4 %1, %2, %3 offen sc1 lds" ::"s"(ldsb), "v"(voff), "s"(r), "s"(soff) : "memory");
}
DEV void dma1n(rsrc_t r, unsigned voff, unsigned soff, unsigned ldsb) {
  asm volatile("s_mov_b32 m0, %0\n\ts_nop 3\n\tbuffer_load_dwordx4 %1, %2, %3 offen nt lds" ::"s"(ldsb), "v"(voff), "s"(r), "s"(soff) : "memory");
}
#define DMA4(P, B) asm volatile("s_mov_b32 m0, %0\n\t" B "\n\t" \
               "buffer_load_dwordx4 %1, %5, %6 offen" P " lds\n\tbuffer_load_dwordx4 %2, %5, %6 offen offset:1024" P " lds\n\t" \
               "buffer_load_dwordx4 %3, %5, %6 offen offset:2048" P " lds\n\tbuffer_load_dwordx4 %4, %5, %6 offen offset:3072" P " lds" \
               ::"s"(ldsb), "v"(v[0]), "v"(v[1]), "v"(v[2]), "v"(v[3]), "s"(r), "s"(soff) : "memory")
template <int POL, bool BAR = false> DEV void dma4x(rsrc_t r, const unsigned (&v)[4], unsigned soff, unsigned ldsb) {
  if constexpr (BAR) { if constexpr (POL == 0) DMA4(" sc1", "s_barrier"); else if constexpr (POL == 1) DMA4(" nt", "s_barrier"); else DMA4("", "s_barrier"); }
  else { if constexpr (POL == 0) DMA4(" sc1", "s_nop 0"); else if constexpr (POL == 1) DMA4(" nt", "s_nop 0"); else DMA4("", "s_nop 0"); }
}
#define PX 2
#define PW 2
#define PD 2
#define P0 2
#define PS 2
DEV void dmaw(rsrc_t r, unsigned voff, unsigned ldsb) {
  asm volatile("s_mov_b32 m0, %0\n\ts_nop 3\n\tbuffer_load_dword %1, %2, 0 offen sc1 lds" ::"s"(ldsb), "v"(voff), "s"(r) : "memory");
}
#define PFP ""
DEV void warm(rsrc_t r, unsigned voff, unsigned soff) {
  asm volatile("s_mov_b32 m0, %0\n\ts_nop 3\n\tbuffer_load_dword %1, %2, %3 offen" PFP " lds" ::"s"(PAD_OFF), "v"(voff), "s"(r), "s"(soff) : "memory");
}
#define WSM 0
DEV void warmq(rsrc_t r, unsigned voff, unsigned soff) {
  warm(r, voff, soff);
}
constexpr int WQ = WSM == 1 ? 2 : 1;
DEV rsrc_t mk_rsrc(const void* p, unsigned bytes) { return __builtin_amdgcn_make_buffer_rsrc((void*)p, (short)0, (int)bytes, 0x00020000); }
DEV bf16x8 ldf(unsigned o) { return *(const __attribute__((address_space(3))) bf16x8*)(size_t)o; }
DEV unsigned vadd(unsigned s, unsigned v) { unsigned r; asm volatile("v_add_u32 %0, %1, %2" : "=v"(r) : "s"(s), "v"(v)); return r; }
DEV bf16x8 ldtr(unsigned o) {
  s16x4 lo = __builtin_amdgcn_ds_read_tr16_b64_v4i16((__attribute__((address_space(3))) s16x4*)(size_t)o);
  s16x4 hi = __builtin_amdgcn_ds_read_tr16_b64_v4i16((__attribute__((address_space(3))) s16x4*)(size_t)(o + 512));
  s16x8 v = __builtin_shufflevector(lo, hi, 0, 1, 2, 3, 4, 5, 6, 7);
  return __builtin_bit_cast(bf16x8, v);
}
DEV f32x4 mma(bf16x8 a, bf16x8 b, f32x4 c) { return __builtin_amdgcn_mfma_f32_16x16x32_bf16(a, b, c, 0, 0, 0); }
DEV unsigned pk2(float a, float b) { f32x2 v = {a, b}; return __builtin_bit_cast(unsigned, __builtin_convertvector(v, bf16x2)); }
DEV u32x4 pk8(f32x4 a, f32x4 b) { u32x4 r = {pk2(a[0], a[1]), pk2(a[2], a[3]), pk2(b[0], b[1]), pk2(b[2], b[3])}; return r; }
DEV float bflo(unsigned v) { return __builtin_bit_cast(float, v << 16); }
DEV float bfhi(unsigned v) { return __builtin_bit_cast(float, v & 0xffff0000u); }
template <int IMM = 0> DEV void st16(u32x4 v, rsrc_t r, unsigned off, unsigned soff) {
  asm volatile("buffer_store_dwordx4 %0, %1, %2, %3 offen offset:%4 sc1\n\ts_nop 1" ::"v"(v), "v"(off), "s"(r), "s"(soff), "n"(IMM));
}
DEV void st8(u32x2 v, rsrc_t r, unsigned off, unsigned soff) { __builtin_amdgcn_raw_buffer_store_b64(v, r, off, soff, 16); }
template <int NM, int NR> DEV void interleave() {
#pragma unroll
  for (int i = 0; i < NR; ++i) {
    __builtin_amdgcn_sched_group_barrier(0x008, 1, 0);
    __builtin_amdgcn_sched_group_barrier(0x100, 1, 0);
  }
  if constexpr (NM > NR) __builtin_amdgcn_sched_group_barrier(0x008, NM - NR, 0);
}
#define STAMP(i) do {} while (0)
#define DST(i) do {} while (0)

DEV f32x4 glu4(f32x4 G, f32x4 U, float ez) {
  f32x4 r;
#pragma unroll
  for (int j = 0; j < 4; ++j) r[j] = U[j] * __builtin_amdgcn_rcpf(__builtin_fmaf(__builtin_amdgcn_exp2f(G[j] * -LOG2E), ez, ez));
  return r;
}
DEV float expz(float z) {
  return __builtin_amdgcn_exp2f(fminf(fmaxf(z, -50.0f), 50.0f) * LOG2E);
}
typedef volatile __attribute__((address_space(3))) float* lds_f;
DEV void ez_put(unsigned zo, float e0, float e1) { *(lds_f)(size_t)zo = e0; *(lds_f)(size_t)(zo + 64) = e1; }
DEV void ez_get(unsigned zo, float& e0, float& e1) {
#pragma unroll 1
  for (unsigned c = 0; c < SPIN_MAX; ++c) {
    e0 = *(lds_f)(size_t)zo; e1 = *(lds_f)(size_t)(zo + 64);
    if (__all(fminf(e0, e1) > 0.0f)) break;
  }
}
DEV int rdy_now() {
  int s; unsigned v;
  asm volatile("ds_read_b32 %1, %2\n\ts_waitcnt lgkmcnt(0)\n\tv_readfirstlane_b32 %0, %1" : "=s"(s), "=&v"(v) : "v"(RDY_OFF) : "memory");
  return s;
}
template <int LVL> DEV void wait_rdy() {
  unsigned s, c, v;
  asm volatile("s_mov_b32 %1, %4\n"
               "1:\n\tds_read_b32 %2, %3\n\ts_waitcnt lgkmcnt(0)\n\tv_readfirstlane_b32 %0, %2\n\ts_cmp_ge_i32 %0, %5\n\ts_cbranch_scc1 2f\n\t"
               "s_sleep 1\n\ts_sub_u32 %1, %1, 1\n\ts_cbranch_scc0 1b\n2:"
               : "=&s"(s), "=&s"(c), "=&v"(v) : "v"(RDY_OFF), "n"(SPIN_MAX), "n"(LVL) : "memory", "scc");
}
template <int B, int N = 8> DEV void waitd(int n) {
  if constexpr (N == 1) waitv<B>();
  else { if (n < N / 2) waitd<B, N / 2>(n); else waitd<B + N / 2, N - N / 2>(n - N / 2); }
}
constexpr int JQE = 5, JQO = 6;
#define OP 0
#define QA 2
#define QB 2
#define SL13 0
#define AP 1
#define ESL 1
#define WE1 0
#define WE2 6
#define GL0 8
#define WO1 0
#define WO2 4
#define TE0 03000301211u
#define TE1 00000001011u
#define TO0 03000020201u
#define TO1 00002010101u
#define TO2 00002000000u
#define TC3 4
constexpr unsigned osum(unsigned t) { return t ? (t & 7) + osum(t >> 3) : 0; }
DEV unsigned nexts(unsigned sb) { return sb == 3 * SLOT_B ? 0u : sb + SLOT_B; }
DEV unsigned nexta(unsigned sb) { return sb == 2 * SLOT_A ? 0u : sb + SLOT_A; }

template <int LW, int PA, int PB, unsigned GA, unsigned GB, bool FIVE = true, bool BAR = false>
DEV void issue(rsrc_t ra, const unsigned (&va)[4], unsigned sa, rsrc_t rb, const unsigned (&vb)[4], unsigned sc, unsigned sb) {
  if constexpr (LW == 0) {
    dma4x<PA, BAR>(ra, va, sa, sb);
#pragma unroll
    for (int g = 1; g < 3; ++g) dma4x<PA>(ra, va, sa + g * GA, sb + g * 4096);
  } else if constexpr (LW == 1) {
    dma4x<PA, BAR>(ra, va, sa + 3 * GA, sb + 3 * 4096);
#pragma unroll
    for (int g = 0; g < 2; ++g) dma4x<PB>(rb, vb, sc + g * GB, sb + 16384 + g * 4096);
  } else {
    dma4x<PB, BAR>(rb, vb, sc + 2 * GB, sb + 16384 + 2 * 4096);
#pragma unroll
    for (int g = 3; g < (FIVE ? 5 : 4); ++g) dma4x<PB>(rb, vb, sc + g * GB, sb + 16384 + g * 4096);
  }
}
template <int LW, int W, int PA, int PB, unsigned GA, unsigned GB, unsigned DA, unsigned DC, bool FIVE = true>
DEV void run(int n, rsrc_t ra, const unsigned (&va)[4], unsigned& sa, rsrc_t rb, const unsigned (&vb)[4], unsigned& sc, unsigned& sb) {
#pragma unroll 1
  for (; n > 0; --n) {
    waitv<W>();
    issue<LW, PA, PB, GA, GB, FIVE, true>(ra, va, sa, rb, vb, sc, sb);
    sa += DA; sc += DC; sb = nexts(sb);
  }
}
template <int LW> DEV void loader(const char* x, const char* wsq, const char* wd, const char* wg, const char* wu, rsrc_t rws,
                                  int lane, int rt, int ct, int kq, char* wsb) {
  const int wid = 4 + LW; (void)wid; (void)wsb;
  STAMP(1);
  const int odd = kq & 1, mm = kq >> 1, fg = 4 * mm + ct;
  const int lr = lane >> 3, lp = lane & 7;
  const unsigned chT = (lp ^ (lr & 6)) * 16;
  const int n3 = odd ? 32 - N3A : N3A, s30 = odd ? N3A : 0;
  const rsrc_t rq = mk_rsrc(wsq, SEQ * SEQ * 2);
  unsigned sb = 0;
  rsrc_t rT; unsigned vT, sT, dT, tb;
  int t1 = 0, t2 = 0, t3 = 0;
#define WARMS(Q) do { t3 = t2; t2 = t1; t1 = tb & 7; tb >>= 3; \
    _Pragma("unroll 1") for (int i_ = t1; i_ > 0; --i_) { if (Q) warmq(rT, vT, sT); else warm(rT, vT, sT); sT += dT; } \
    if (Q) t1 *= WQ; } while (0)
#define WARM_Q() do { rT = rq; vT = (lane & 31) * (SEQ * 2) + (lane >> 5) * 128; sT = (rt * 128 + ct * 32) * (SEQ * 2) + s30 * 128; dT = 256; } while (0)
  {
    unsigned vx[4], vw[4], vp[4];
#pragma unroll
    for (int j = 0; j < 4; ++j) {
      vx[j] = (lr + 8 * j) * (DM * 2) + chT - j * 1024;
      vw[j] = (lr + 8 * j) * (FF * 2) + (((lp >> 1) ^ (((lr >> 1) & 1) | ((j & 1) << 1))) * 2 + (lp & 1)) * 16 - j * 1024;
      vp[j] = (8 * (lane >> 5) + (lr & 3) + 16 * (j & 1) + 4 * (j >> 1)) * (DM * 2) + chT - j * 1024;
    }
    const rsrc_t rx = mk_rsrc(x, SEQ * DM * 2);
    const rsrc_t rw = mk_rsrc(LW == 2 ? wu : wg, DM * FF * 2);
    if constexpr (LW == 0) { rT = rx; vT = (lane & 31) * (DM * 2) + (lane >> 5) * 128; sT = (rt * 128 + ct * 32) * (DM * 2) + 128; dT = 256; }
    if (!odd) {
      if constexpr (LW != 0) {
        rT = rw; vT = ((lane >> 4) * 64 + ((lane & 15) >> 1)) * (FF * 2) + (lane & 1) * 128;
        sT = (64 + (rt & 7) * 8) * (FF * 2) + fg * 256; dT = 256 * (FF * 2);
      }
      tb = LW == 0 ? TE0 : TE1;
      WARMS(false);
      unsigned so = LW == 0 ? rt * 128 * (DM * 2) + 128 : fg * 256 + 64 * (FF * 2);
#pragma unroll 1
      for (int j = 1; j < 10; ++j) {
        if (j == 2) STAMP(2);
        if (j >= 2) { waitd<16>(t1 + t2); bar(); }
        if constexpr (LW == 0) {
#pragma unroll
          for (int g = 0; g < 4; ++g) dma4x<PX>(rx, vx, so + g * 32 * (DM * 2), sb + g * 4096);
          so += 128;
          if (j == JQE) WARM_Q();
        } else {
#pragma unroll
          for (int g = 0; g < 4; ++g) dma4x<PW>(rw, vw, so + (g & 1) * 32 * (FF * 2) + (g >> 1) * 128, sb + LW * 16384 + g * 4096);
          so += 64 * (FF * 2);
        }
        sb = nexta(sb);
        WARMS(LW == 0 && j >= JQE);
      }
      waitd<16>(t1 + t2); bar();
      sb = 0;
    } else {
      const rsrc_t rd = mk_rsrc(wd, DM * DM * 2);
      const unsigned vg = ((lane >> 3) * 64 + (lane & 7)) * (FF * 2), sg = (rt & 7) * 8 * (FF * 2) + 2048 + fg * 128;
      if constexpr (LW == 1) {
        const int l20 = (lane * 205) >> 12;
        rT = rd; vT = lane < 60 ? (lane - l20 * 20) * (DM * 2) + l20 * 128 : 0x80000000u;
        sT = (ct * 160 + (rt & 7) * 20) * (DM * 2) + 128; dT = 384;
      }
      if constexpr (LW == 2) { rT = rw; vT = vg; sT = sg; dT = 512 * (FF * 2); }
      tb = LW == 0 ? TO0 : LW == 1 ? TO1 : TO2;
      WARMS(false);
      unsigned sa = rt * 128 * (DM * 2) + 128, sc = ct * 160 * (DM * 2) + 128;
      sb = SLOT_B;
#pragma unroll 1
      for (int j = 1; j < 10; ++j) {
        if (j == 3) STAMP(2);
        if (j >= 3) { waitd<24>(t1 + t2 + t3); bar(); }
        issue<LW, PX, PD, 32 * (DM * 2), 32 * (DM * 2)>(rx, vp, sa, rd, vx, sc, sb);
        sa += 128; sc += 128; sb = nexts(sb);
        if constexpr (LW == 0) { if (j == JQO) WARM_Q(); }
        if constexpr (LW == 1) { if (j == 5) { rT = rw; vT = vg; sT = sg; dT = 512 * (FF * 2); } }
        WARMS(LW == 0 && j >= JQO);
      }
      sa = rt * 128 * (DM * 2); sc = 2048 + fg * 128 - (LW == 2 ? 64 * (FF * 2) : 0);
      constexpr unsigned TL = LW == 0 ? TO0 : LW == 1 ? TO1 : TO2;
      static_assert(((TL >> 21) & 077) == 0, "no warm-up behind stages 7 and 8");
      constexpr int T9 = ((TL >> 27) & 7) * (LW == 0 ? WQ : 1), W0 = LW == 2 ? 16 : 24;
      if constexpr (T9 > 0) run<LW, W0 + T9, PX, PW, 32 * (DM * 2), 32 * (FF * 2), 128, 64 * (FF * 2), false>(3, rx, vx, sa, rw, vw, sc, sb);
      run<LW, W0, PX, PW, 32 * (DM * 2), 32 * (FF * 2), 128, 64 * (FF * 2), false>(T9 > 0 ? 7 : 10, rx, vx, sa, rw, vw, sc, sb);
    }
  }
  const unsigned vl[4] = {lane * 16u, lane * 16u, lane * 16u, lane * 16u};
  {
    unsigned vs[4];
#pragma unroll
    for (int k = 0; k < 4; ++k) vs[k] = (lr + 8 * k) * (SEQ * 2) + chT - k * 1024;
    unsigned sa = rt * 128 * (SEQ * 2) + s30 * 128, sc = WS_AB + ((mm * 4 + ct) * 32 + s30) * 20480;
#define RUN_Y(W, N) run<LW, W, PS, 0, 32 * (SEQ * 2), 4096, 128, 20480>(N, rq, vs, sa, rws, vl, sc, sb)
    int nr = n3 - 4;
    if (odd) {
      if constexpr (LW == 0) {
        RUN_Y(24, 4);
        WARM_Q();
        sT += osum(TO0 >> (3 * JQO)) * 256;
#pragma unroll 1
        for (int i = 0; i < TC3; ++i) { warmq(rT, vT, sT); sT += dT; }
        RUN_Y(24 + TC3 * WQ, 3);
        nr = n3 - 7;
      } else if constexpr (LW == 2) {
        RUN_Y(16, 2);
        nr = n3 - 2;
      } else nr = n3;
    } else {
      int pb = 2;
#pragma unroll 1
      for (int j = 0; j < 4; ++j) {
        while (pb > 0 && (j >= 3 - pb || (LW != 0 && rdy_now() < 1))) {
          constexpr int T9 = LW == 0 ? ((TE0 >> 27) & 7) * WQ : (TE1 >> 27) & 7;
          if (pb == 2) { if (j > 0) waitv<12 + T9>(); else waitv<T9>(); }
          bar(); --pb;
        }
        if (j == 2) { waitv<12>(); bar(); }
        if (LW != 0 && j == 0) wait_rdy<1>();
        issue<LW, PS, 0, 32 * (SEQ * 2), 4096>(rq, vs, sa, rws, vl, sc, sb);
        sa += 128; sc += 20480; sb = nexts(sb);
      }
    }
    RUN_Y(24, nr);
  }
  {
    unsigned sa = WS_H1 + (rt * 4 + kq) * NS * 16384, sc = WS_W1 + (ct * 4 + kq) * NS * 20480;
#pragma unroll 1
    for (int ph = 0; ph < 2; ++ph) {
      run<LW, 24, 0, 0, 4096, 4096, 16384, 20480>(ph ? NT : NS, rws, vl, sa, rws, vl, sc, sb);
      sa = WS_H2 + (rt * 2 + mm) * NT * 16384; sc = WS_W2 + (ct * 2 + odd) * NT * 20480;
    }
  }
  STAMP(7);
  waitv<24>(); STAMP(8); bar();
  waitv<12>(); STAMP(9); bar();
  waitv<0>(); STAMP(10); bar();
  STAMP(11);
  bar();
  STAMP(12);
}

__global__ __launch_bounds__(512, 2) void fused_kernel(const char*, const char*, const char*, const char*, const char*, const char*, const char*,
                                                       const char*, const char*, char*, char*) {
  u32x16 ka; u32x4 kb; u32x2 kc;
  asm volatile("s_load_dwordx16 %0, %3, 0x0\n\ts_load_dwordx4 %1, %3, 0x40\n\ts_load_dwordx2 %2, %3, 0x50"
               : "=&s"(ka), "=&s"(kb), "=&s"(kc) : "s"(__builtin_amdgcn_kernarg_segment_ptr()) : "memory");
  extern __shared__ __attribute__((aligned(16))) char smem_raw[];
  const lds_p sm = (lds_p)smem_raw;
  const int wid = __builtin_amdgcn_readfirstlane(threadIdx.x >> 6), lane = threadIdx.x & 63;
  const int bid = blockIdx.x, xcd = bid & 7, idx = bid >> 3;
  const int kq = xcd >> 1, rt = (xcd & 1) * 8 + (idx >> 2), ct = idx & 3;
  const int odd = kq & 1, mm = kq >> 1, fg = 4 * mm + ct;
  if (wid == 7) warm(mk_rsrc((const char*)(__builtin_amdgcn_s_getpc() & ~127ull), CODE_BYTES), ct * ((CODE_BYTES - 8192 + 383) / 384 * 128) + lane * 128, 0);
  const int b2 = odd ? 20 : 10, n3 = odd ? 32 - N3A : N3A, s30 = odd ? N3A : 0;
  const int b3 = b2 + n3, b35 = b3 + NS, b4 = b35 + NT;
#define HPE 7
#define HPO 5
  const int HP = odd ? HPO : HPE;
  const int lw = wid - 4;
  const bool isw = !odd && lw > 0, isd = odd && lw > 0, isp = odd && lw == 0;
  unsigned v0[4];
  {
    const int lr = lane >> 3, lp = lane & 7;
    const unsigned c0 = (lp ^ (lr & (isw ? 2 : 6))) * 16;
    const unsigned rb = ((lr & (isp ? 3 : 7)) + (lr & (isp ? 4 : 0)) * 2) * (isw ? FF * 2 : DM * 2);
    const unsigned k1 = isw ? 23552 : isp ? 19456 : 9216, k2 = isp ? 3072 : 2 * k1;
    v0[0] = rb + c0; v0[1] = rb + (c0 ^ (isw ? 64 : 0)) + k1; v0[2] = v0[0] + k2; v0[3] = v0[1] + k2;
  }
  asm volatile("" : "+v"(v0[0]), "+v"(v0[1]), "+v"(v0[2]), "+v"(v0[3]));
  asm volatile("s_waitcnt lgkmcnt(0)" : "+s"(ka), "+s"(kb), "+s"(kc)::"memory");
#define KARG(K, I) ((char*)(gptr)(((unsigned long long)K[2 * (I) + 1] << 32) | K[2 * (I)]))
  const char *x = KARG(ka, 0), *wsq = mm ? KARG(ka, 2) : KARG(ka, 1), *wd = mm ? KARG(ka, 4) : KARG(ka, 3), *wg = KARG(ka, 5), *wu = KARG(ka, 6);
  const char *wdn = KARG(ka, 7), *gate_w = KARG(kb, 0);
  char *out = KARG(kb, 1), *wsb = KARG(kc, 0);
  const rsrc_t rws = mk_rsrc(wsb, WS_END);
  if (lw >= 0 && lw < 3) {
    const rsrc_t r = mk_rsrc(isw ? (lw == 1 ? wg : wu) : isd ? wd : x, isw ? DM * FF * 2 : isd ? DM * DM * 2 : SEQ * DM * 2);
    const unsigned s0 = isw ? fg * 256 : (isd ? ct * 160 : rt * 128) * (DM * 2);
    const unsigned ga = isw ? 32 * (FF * 2) : 32 * (DM * 2), gb = isw ? 128 : 64 * (DM * 2);
    const unsigned lb = odd ? (lw ? 16384 : 0) : 2 * SLOT_A + lw * 16384;
    const int g1 = isd ? 3 * lw - 1 : 4;
#pragma unroll 1
    for (int g = isd ? 2 * lw - 2 : 0; g < g1; ++g) dma4x<P0>(r, v0, s0 + (g & 1) * ga + (g >> 1) * gb, lb + g * 4096);
  }
  STAMP(0);

  if (wid < 4) {
    const int r16 = lane & 15, q = lane >> 4;
    const unsigned t0 = r16 * 128 + ((q ^ (r16 & 6)) << 4), t1 = t0 ^ 64;
    const int q4 = r16 >> 2, fx = ((q4 >> 1) & 1) | ((q & 1) << 1);
    const unsigned xrow = (8 * q + q4) * 128 + (lane & 3) * 8;
    const int hi = wid >> 1, lo = wid & 1;
    const unsigned l16 = lane * 16, zo = Z_OFF + (wid * 32 + r16) * 4;
    if (!odd) {
      f32x4 acc[8][4] = {}, zc[2] = {};
      bf16x8 xa[4], wa[9], xb[4], wb[9];
      unsigned xo[4], po[4];
#pragma unroll
      for (int m = 0; m < 4; ++m) { xo[m] = xrow + ((m ^ fx) << 5) + 16384 + lo * 8192; po[m] = xo[m] + 2 * SLOT_A; }
      const unsigned ta = t0 + hi * 8192 + lo * 4096, tc = ta ^ 4096;
      unsigned pa = ta + 2 * SLOT_A, pb = pa ^ 64, pc = tc + 2 * SLOT_A, pd = pc ^ 64, go = GW_OFF + q * 16;
#define RD_GUW(X, W, TP, TQ, KS) do { X[0] = ldf(TP); X[1] = ldf((TP) + 2048); X[2] = ldf(TQ); X[3] = ldf((TQ) + 2048); \
      _Pragma("unroll") for (int m = 0; m < 4; ++m) { W[m] = ldtr(po[m] + (KS) * 4096); W[4 + m] = ldtr(po[m] + 16384 + (KS) * 4096); } \
      W[8] = ldf(go + (KS) * 64); } while (0)
#define MM_GUW(X, W, K0, K1) do { _Pragma("unroll") for (int k = (K0); k < (K1); ++k) { \
      if (k < 32) acc[k >> 2][k & 3] = mma(W[k >> 2], X[k & 3], acc[k >> 2][k & 3]); else zc[k & 1] = mma(W[8], X[k & 1], zc[k & 1]); } } while (0)
      bar();
      STAMP(1);
      unsigned sb = 2 * SLOT_A;
      RD_GUW(xa, wa, pa, pc, 0);
ALN(KAL);
#pragma unroll 1
      for (int n = 0; n < 10; ++n) {
        SCHED0;
        RD_GUW(xb, wb, pb, pd, 1);
        MM_GUW(xa, wa, 0, 22);
        interleave<22, 21>();
        SCHED0;
        sb = nexta(sb);
#pragma unroll
        for (int m = 0; m < 4; ++m) { MM_GUW(xa, wa, 22 + m, 23 + m); SCHED0; po[m] = vadd(sb, xo[m]); SCHED0; }
        MM_GUW(xa, wa, 26, 27); SCHED0; pa = vadd(sb, ta); SCHED0;
        MM_GUW(xa, wa, 27, 28); SCHED0; pc = vadd(sb, tc); SCHED0;
        MM_GUW(xa, wa, 28, 29); SCHED0; pb = vadd(sb, ta ^ 64); SCHED0;
        MM_GUW(xa, wa, 29, 30); SCHED0; pd = vadd(sb, tc ^ 64); SCHED0;
        MM_GUW(xa, wa, 30, 31); SCHED0; go = vadd(128, go); SCHED0;
        MM_GUW(xa, wa, 31, 34);
        SCHED0;
        wait_lds(); bar(); DST(n);
        RD_GUW(xa, wa, pa, pc, 0);
        MM_GUW(xb, wb, 0, 34);
        interleave<34, 21>();
      }
      SCHED0;
      STAMP(2);
      float ez[4] = {expz(zc[0][0]), expz(zc[1][0])};
      ez_put(zo, ez[0], ez[1]);
      const bool h1 = fg < 2 * NS;
      const unsigned hbase = h1 ? WS_H1 + ((rt * 4 * NS + fg * 2 + lo) * 16 + hi * 8) * 1024
                                : WS_H2 + (((rt * 2 + hi) * NT + fg - 2 * NS) * 16 + lo * 8) * 1024;
      const unsigned si = h1 ? 2048 : 1024, hv[2] = {l16, l16 + (h1 ? 1024 : 4096)};
#pragma unroll
      for (int i = 0; i < 4; ++i) {
        if (i == 2) ez_get(zo ^ 128, ez[2], ez[3]);
#pragma unroll
        for (int p = 0; p < 2; ++p)
          st16(pk8(glu4(acc[2 * p][i], acc[4 + 2 * p][i], ez[i]), glu4(acc[2 * p + 1][i], acc[5 + 2 * p][i], ez[i])), rws, hv[p],
               hbase + (i ^ (2 * lo)) * si);
      }
    } else {
      unsigned sb = 0;
      {
        f32x4 acc[4][5] = {};
        bf16x8 xa[4], wa[5], xb[4], wb[5];
        const unsigned ta = t0 + hi * 8192, ua = t0 + 16384 + lo * 10240;
        unsigned pa = ta, pu = ua;
#define RD_TT(X, W, TP, UP) do { _Pragma("unroll") for (int i = 0; i < 4; ++i) X[i] = ldf((TP) + i * 2048); \
        _Pragma("unroll") for (int j = 0; j < 5; ++j) W[j] = ldf((UP) + j * 2048); } while (0)
#define MM_AB(X, W, K0, K1) do { _Pragma("unroll") for (int k = (K0); k < (K1); ++k) acc[k & 3][k >> 2] = mma(X[k & 3], W[k >> 2], acc[k & 3][k >> 2]); } while (0)
        bar();
        STAMP(1);
        RD_TT(xa, wa, pa, pu);
        unsigned pb = pa ^ 64, pv = pu ^ 64;
ALN(KAL);
#pragma unroll 1
        for (int n = 0; n < 10; ++n) {
          SCHED0;
          RD_TT(xb, wb, pb, pv);
          MM_AB(xa, wa, 0, 11);
          interleave<11, 9>();
          SCHED0;
          sb = nexts(sb);
          MM_AB(xa, wa, 11, 12); SCHED0; pa = vadd(sb, ta); SCHED0;
          MM_AB(xa, wa, 12, 13); SCHED0; pu = vadd(sb, ua); SCHED0;
          MM_AB(xa, wa, 13, 14); SCHED0; pb = vadd(sb, ta ^ 64); SCHED0;
          MM_AB(xa, wa, 14, 15); SCHED0; pv = vadd(sb, ua ^ 64); SCHED0;
          MM_AB(xa, wa, 15, 20);
          SCHED0;
          wait_lds(); bar(); DST(n);
          RD_TT(xa, wa, pa, pu);
          MM_AB(xb, wb, 0, 20);
          interleave<20, 9>();
        }
        SCHED0;
        const unsigned abase = WS_AB + ((mm * 4 + ct) * 32 + rt * 2 + hi) * 20480 + lo * 10240;
        const unsigned sg = mm ? 0x80008000u : 0u;
#pragma unroll
        for (int j = 0; j < 5; ++j) {
          st16<0>(pk8(acc[0][j], acc[1][j]) ^ sg, rws, l16, abase + j * 2048);
          st16<1024>(pk8(acc[2][j], acc[3][j]) ^ sg, rws, l16, abase + j * 2048);
        }
      }
      STAMP(2);
      {
        f32x4 acc[4][4] = {}, zc[2] = {};
        bf16x8 xa[4], wa[5], xb[4], wb[5];
        unsigned xo[2], po[2];
#pragma unroll
        for (int m = 0; m < 2; ++m) { xo[m] = xrow + (((2 * lo + m) ^ fx) << 5) + 16384; po[m] = xo[m] + sb; }
        const unsigned ta = t0 + hi * 8192 + lo * 4096, tc = ta ^ 4096;
        unsigned pa = ta + sb, pb = (ta ^ 64) + sb, pc = tc + sb, pd = (tc ^ 64) + sb, go = GW_OFF + q * 16;
#define RD_GUN(X, W, TP, TQ, KS) do { X[0] = ldf(TP); X[1] = ldf((TP) + 2048); X[2] = ldf(TQ); X[3] = ldf((TQ) + 2048); \
        _Pragma("unroll") for (int m = 0; m < 2; ++m) { W[m] = ldtr(po[m] + (KS) * 4096); W[2 + m] = ldtr(po[m] + 8192 + (KS) * 4096); } \
        W[4] = ldf(go + (KS) * 64); } while (0)
#define MM_GUN(X, W, K0, K1) do { _Pragma("unroll") for (int k = (K0); k < (K1); ++k) { \
        if (k < 16) acc[k >> 2][k & 3] = mma(W[k >> 2], X[k & 3], acc[k >> 2][k & 3]); else zc[k & 1] = mma(W[4], X[k & 1], zc[k & 1]); } } while (0)
        RD_GUN(xa, wa, pa, pc, 0);
#pragma unroll 1
        for (int pass = 0; pass < 2; ++pass) {
ALN(KAL);
#pragma unroll 1
          for (int n = pass ? 10 - AP : AP; n > 0; --n) {
            SCHED0;
            RD_GUN(xb, wb, pb, pd, 1);
            MM_GUN(xa, wa, 0, 13);
            interleave<13, 13>();
            SCHED0;
            sb = nexts(sb);
            MM_GUN(xa, wa, 13, 14); SCHED0; pa = vadd(sb, ta); po[0] = vadd(sb, xo[0]); SCHED0;
            MM_GUN(xa, wa, 14, 15); SCHED0; po[1] = vadd(sb, xo[1]); pc = vadd(sb, tc); SCHED0;
            MM_GUN(xa, wa, 15, 16); SCHED0; pb = vadd(sb, ta ^ 64); go = vadd(128, go); SCHED0;
            MM_GUN(xa, wa, 16, 17); SCHED0; pd = vadd(sb, tc ^ 64); SCHED0;
            MM_GUN(xa, wa, 17, 18);
            SCHED0;
            wait_lds(); bar(); DST(pass ? 20 - n : 10 + AP - n);
            RD_GUN(xa, wa, pa, pc, 0);
            MM_GUN(xb, wb, 0, 18);
            interleave<18, 13>();
          }
          SCHED0;
          if (pass == 0) {
            waitv<0>();
            if (lane == 0)
              __hip_atomic_store((gu32*)(wsb + FL_AB + (mm * 4 + ct) * 256 + rt * 16 + wid * 4), *(volatile lds_u32*)(sm + TOK_OFF), __ATOMIC_RELAXED,
                                 __HIP_MEMORY_SCOPE_AGENT);
          }
        }
        float ez[4] = {expz(zc[0][0]), expz(zc[1][0])};
        ez_put(zo, ez[0], ez[1]);
        const int fb = 32 + fg * 2 + lo - 8 * NS;
        const unsigned hbase = WS_H2 + (((rt * 2 + hi) * NT + (fb >> 2)) * 16 + (fb & 3) * 4) * 1024;
#pragma unroll
        for (int i = 0; i < 4; ++i) {
          if (i == 2) ez_get(zo ^ 128, ez[2], ez[3]);
          st16(pk8(glu4(acc[0][i], acc[2][i], ez[i]), glu4(acc[1][i], acc[3][i], ez[i])), rws, l16, hbase + (i ^ (2 * lo)) * 1024);
        }
      }
    }
    STAMP(3);
    unsigned sb = 0;
    {
      f32x4 acc[5][4] = {};
      bf16x8 xa[4], wa[5], xb[4], wb[5];
      unsigned ta = t0 + hi * 8192, tb = t1 + hi * 8192;
      const unsigned ua = l16 + 16384 + lo * 10240;
      unsigned pa = ta, pb = tb, pu = ua, pv = ua;
#define MM_Y(X, W, K0, K1) do { _Pragma("unroll") for (int k = (K0); k < (K1); ++k) acc[k >> 2][k & 3] = mma(W[k >> 2], X[k & 3], acc[k >> 2][k & 3]); } while (0)
      if (!odd) bar();
      STAMP(4);
      RD_TT(xa, wa, pa, pu);
      int dn = b2;
#pragma unroll 1
      for (int pass = 0; pass < 3; ++pass) {
#pragma unroll 1
        for (int n = pass == 0 ? HP : pass == 1 ? n3 - 1 - HP : NS + 1; n > 0; --n) {
          SCHED0;
          RD_TT(xb, wb, pb, pv + 1024);
          MM_Y(xa, wa, 0, 11);
          interleave<11, 9>();
          SCHED0;
          sb = nexts(sb);
          MM_Y(xa, wa, 11, 12); SCHED0; pa = vadd(sb, ta); SCHED0;
          MM_Y(xa, wa, 12, 13); SCHED0; pu = vadd(sb, ua); SCHED0;
          MM_Y(xa, wa, 13, 14); SCHED0; pb = vadd(sb, tb); SCHED0;
          MM_Y(xa, wa, 14, 20);
          SCHED0;
          wait_lds(); bar(); DST(dn); ++dn;
          RD_TT(xa, wa, pa, pu);
          MM_Y(xb, wb, 0, 20);
          interleave<20, 9>();
          pv = pu;
        }
        SCHED0;
        waitv<0>();
        if (pass) { ta = l16 + hi * 8192; tb = ta + 1024; }
        asm volatile("" : "+v"(ta), "+v"(tb));
      }
      SCHED0;
      STAMP(6);
      if (wid == kq) {
#pragma unroll
        for (int i = 0; i < 4; ++i)
#pragma unroll
          for (int j = 0; j < 5; ++j) {
            u32x2 v = {pk2(acc[j][i][0], acc[j][i][1]), pk2(acc[j][i][2], acc[j][i][3])};
            *(__attribute__((address_space(3))) u32x2*)(sm + SPARE + i * 2560 + j * 512 + lane * 8) = v;
          }
      } else {
        const unsigned sbase = WS_SL + (((rt * 4 + ct) * 4 + wid) * 3 + ((kq - wid - 1) & 3)) * 10240;
#pragma unroll
        for (int i = 0; i < 4; ++i) {
          st16<0>(pk8(acc[0][i], acc[1][i]), rws, l16, sbase + i * 2560);
          st16<1024>(pk8(acc[2][i], acc[3][i]), rws, l16, sbase + i * 2560);
          u32x2 v = {pk2(acc[4][i][0], acc[4][i][1]), pk2(acc[4][i][2], acc[4][i][3])};
          st8(v, rws, lane * 8 + i * 2560 + 2048, sbase);
        }
      }
    }
    {
      f32x4 o[5] = {};
      bf16x8 ha[2], wa[10], hb[2], wb[10];
      const unsigned ho = l16 + wid * 1024, wo = l16 + 16384;
#define RD_O(H, W, SB, HF) do { _Pragma("unroll") for (int k = 0; k < 2; ++k) { H[k] = ldf((SB) + ho + (2 * (HF) + k) * 4096); \
      _Pragma("unroll") for (int j = 0; j < 5; ++j) W[k * 5 + j] = ldf((SB) + wo + ((2 * (HF) + k) * 5 + j) * 1024); } } while (0)
#define MM_O(H, W) do { _Pragma("unroll") for (int k = 0; k < 2; ++k) _Pragma("unroll") for (int j = 0; j < 5; ++j) o[j] = mma(W[k * 5 + j], H[k], o[j]); } while (0)
      u32x4 su[3] = {}, sv[3] = {};
      u32x2 sw[3] = {};
      const unsigned sl = WS_SL + ((rt * 4 + ct) * 4 + kq) * 30720 + wid * 2560, l8 = lane * 8;
      int early = 0;
#define LD_SLABS(SKIP) asm volatile("s_cmp_ge_i32 %9, 4\n\t" SKIP " 1f\n\t" \
        "buffer_load_dwordx4 %0, %10, %12, %13 offen sc1\n\tbuffer_load_dwordx4 %3, %10, %12, %13 offen offset:1024 sc1\n\t" \
        "buffer_load_dwordx2 %6, %11, %12, %13 offen offset:2048 sc1\n\t" \
        "buffer_load_dwordx4 %1, %10, %12, %14 offen sc1\n\tbuffer_load_dwordx4 %4, %10, %12, %14 offen offset:1024 sc1\n\t" \
        "buffer_load_dwordx2 %7, %11, %12, %14 offen offset:2048 sc1\n\t" \
        "buffer_load_dwordx4 %2, %10, %12, %15 offen sc1\n\tbuffer_load_dwordx4 %5, %10, %12, %15 offen offset:1024 sc1\n\t" \
        "buffer_load_dwordx2 %8, %11, %12, %15 offen offset:2048 sc1\n1:" \
        : "+v"(su[0]), "+v"(su[1]), "+v"(su[2]), "+v"(sv[0]), "+v"(sv[1]), "+v"(sv[2]), "+v"(sw[0]), "+v"(sw[1]), "+v"(sw[2]) \
        : "s"(early), "v"(l16), "v"(l8), "s"(rws), "s"(sl), "s"(sl + 10240), "s"(sl + 20480) : "memory", "scc")
#define SP 2
#define SL_POST() do { if (wid != kq) { waitv<0>(); \
          if (lane == 0) __hip_atomic_store((gu32*)(wsb + FL_SL + (rt * 4 + ct) * 128 + wid * 16 + ((kq - wid - 1) & 3) * 4), \
                                            *(volatile lds_u32*)(sm + TOK_OFF), __ATOMIC_RELAXED, __HIP_MEMORY_SCOPE_AGENT); } } while (0)
      STAMP(7);
      RD_O(ha, wa, sb, 0);
#pragma unroll
      for (int n = 0; n < NT; ++n) {
        SCHED0;
        if (SP == 1 && n == 1) SL_POST();
        RD_O(hb, wb, sb, 1);
        MM_O(ha, wa);
        SCHED0;
        if (SP != 1 && n == SP / 2) SL_POST();
        wait_lds(); bar(); DST(b35 + n);
        if (ESL && n == NT - 2) { early = rdy_now(); LD_SLABS("s_cbranch_scc0"); }
        sb = nexts(sb);
        if (n + 1 < NT) RD_O(ha, wa, sb, 0);
        MM_O(hb, wb);
      }
      SCHED0;
      STAMP(8);
      if (early < 4) wait_rdy<4>();
      LD_SLABS("s_cbranch_scc1");
      const float ez = *(lds_f)(size_t)(Z_OFF + (mm * 64 + wid * 16 + r16) * 4), gi = ez * __builtin_amdgcn_rcpf(1.0f + ez);
#pragma unroll
      for (int j = 0; j < 5; ++j) {
        const u32x2 v = *(const __attribute__((address_space(3))) u32x2*)(sm + SPARE + wid * 2560 + j * 512 + lane * 8);
        o[j] += (f32x4){bflo(v[0]), bfhi(v[0]), bflo(v[1]), bfhi(v[1])};
      }
      asm volatile("s_waitcnt vmcnt(0)" : "+v"(su[0]), "+v"(su[1]), "+v"(su[2]), "+v"(sv[0]), "+v"(sv[1]), "+v"(sv[2]), "+v"(sw[0]), "+v"(sw[1]), "+v"(sw[2])::"memory");
      STAMP(9);
#pragma unroll
      for (int s = 0; s < 3; ++s) {
        const u32x4 u = su[s], v = sv[s];
        const u32x2 w = sw[s];
        o[0] += (f32x4){bflo(u[0]), bfhi(u[0]), bflo(u[1]), bfhi(u[1])};
        o[1] += (f32x4){bflo(u[2]), bfhi(u[2]), bflo(u[3]), bfhi(u[3])};
        o[2] += (f32x4){bflo(v[0]), bfhi(v[0]), bflo(v[1]), bfhi(v[1])};
        o[3] += (f32x4){bflo(v[2]), bfhi(v[2]), bflo(v[3]), bfhi(v[3])};
        o[4] += (f32x4){bflo(w[0]), bfhi(w[0]), bflo(w[1]), bfhi(w[1])};
      }
      const rsrc_t ro = mk_rsrc(out, SEQ * DM * 2);
      const unsigned op = (wid * 16 + r16) * (DM * 2) + q * 8, os = (rt * 128 + mm * 64) * (DM * 2) + (ct * 160 + odd * 80) * 2;
#pragma unroll
      for (int j = 0; j < 5; ++j) {
        u32x2 v = {pk2(o[j][0] * gi, o[j][1] * gi), pk2(o[j][2] * gi, o[j][3] * gi)};
        __builtin_amdgcn_raw_buffer_store_b64(v, ro, op + j * 32, os, OP);
      }
      STAMP(10);
    }
  } else if (wid < 7) {
    if (wid == 4) loader<0>(x, wsq, wd, wg, wu, rws, lane, rt, ct, kq, wsb);
    else if (wid == 5) loader<1>(x, wsq, wd, wg, wu, rws, lane, rt, ct, kq, wsb);
    else loader<2>(x, wsq, wd, wg, wu, rws, lane, rt, ct, kq, wsb);
  } else {
    volatile lds_u32* rdy = (volatile lds_u32*)(sm + RDY_OFF);
    if (lane == 0) dmaw(rws, WS_FLAG, PK_EP);
    {
      const rsrc_t rg = mk_rsrc(gate_w, DM * 2);
      dma1(rg, lane * 16, 0, GW_OFF);
      dma1(rg, lane * 16, 1024, GW_OFF + 1024);
    }
    const int unit = odd ? (rt < 8 ? 128 + (mm * 8 + rt) * 4 + ct : -1) : (mm * 16 + rt) * 4 + ct;
    const int ufb = unit >> 2, uct = unit & 3;
#define PUBLISH(FOFF) do { if (lane == 0) __hip_atomic_store((gu32*)(wsb + (FOFF)), token, __ATOMIC_RELAXED, __HIP_MEMORY_SCOPE_AGENT); } while (0)
#define BARS(CNT) do { _Pragma("unroll 1") for (int c_ = (CNT); c_ > 0; --c_) bar(); } while (0)
    waitv<0>();
    const unsigned token = __builtin_amdgcn_readfirstlane(*(volatile lds_u32*)(sm + PK_EP)) + 0x9E3779B9u;
    *(volatile lds_u32*)(sm + PK_H + lane * 4) = ~token;
    *(volatile lds_u32*)(sm + PK_AB + lane * 4) = ~token;
    *(volatile lds_u32*)(sm + Z_OFF + lane * 4) = 0u;
    *(volatile lds_u32*)(sm + Z_OFF + 256 + lane * 4) = 0u;
    if (lane < 24) *(volatile lds_u32*)(sm + PK_A + lane * 4) = lane == 22 ? token : (lane == 20 ? 0u : ~token);
    wait_lds();
    const unsigned fa = FL_AB + (mm * 4 + ct) * 256 + (odd ? max(lane >> 2, N3A >> 1) : min(lane >> 2, (N3A - 1) >> 1)) * 16 + (lane & 3) * 4;
    const int l15 = lane & 15, f1 = min((kq * NS >> 1) + l15, (kq * NS + NS - 1) >> 1);
    const unsigned f1o = l15 < 4 ? FL_H + rt * 128 + ((f1 & 3) * 4 + (f1 >> 2) * 2) * 4 : FL_WD + ct * 256 + (kq * NS * 2 + min(l15 - 4, 2 * NS - 1)) * 4;
    const unsigned fh = lane < 16 ? FL_H + rt * 128 + lane * 4 : FL_WD + ct * 256 + (lane - 16) * 4;
    const unsigned fs = FL_SL + (rt * 4 + ct) * 128 + kq * 16 + min(lane & 3, 2) * 4;
    int glim = 64;
#define GATE(CNT, L, FOFF, NL, LOFF, ACT, PRE) do { bool got_ = false; \
      _Pragma("unroll 1") for (int c_ = (CNT);; --c_) { \
        if (!got_) { \
          got_ = __all(*(volatile lds_u32*)(sm + (LOFF) + (lane & ((NL) - 1)) * 4) == token || lane >= glim); \
          if (!got_ && c_ == 0) { \
            _Pragma("unroll 1") for (unsigned spin = 0; spin < SPIN_MAX; ++spin) { \
              if (__all(__hip_atomic_load((const gu32*)(wsb + (FOFF)), __ATOMIC_RELAXED, __HIP_MEMORY_SCOPE_AGENT) == token || lane >= glim)) break; \
              __builtin_amdgcn_s_sleep(1); } \
            got_ = true; } \
          if (got_) { if (lane == 0) *rdy = (L); ACT; wait_lds(); } \
          else if (lane < (NL)) dmaw(rws, (FOFF), (LOFF)); } \
        if (c_ == 0) break; \
        PRE; \
        bar(); } } while (0)
#define WDN_LOAD() do { const rsrc_t rdn = mk_rsrc(wdn, unit >= 0 ? FF * DM * 2 : 0); \
      _Pragma("unroll 1") for (int i = 0; i < 10; ++i) { const int c = i * 64 + lane, row = (c * 3277) >> 16; \
        dma1n(rdn, row * (DM * 2) + (c - row * 20) * 16, ufb * 32 * (DM * 2) + uct * 320, SPARE + i * 1024); } } while (0)
    BARS(odd ? WO1 : WE1);
    WDN_LOAD();
    const rsrc_t rq = mk_rsrc(wsq, SEQ * SEQ * 2);
    const unsigned vq = (lane & 31) * (SEQ * 2) + (lane >> 5) * 128;
    constexpr int Q0 = osum(TE0 >> (3 * JQE)), QN = (N3A + 1) / 2 - Q0;
    const unsigned sq = (rt * 128 + ct * 32) * (SEQ * 2) + Q0 * 256;
    BARS(odd ? WO2 - WO1 : WE2 - WE1);
    waitv<0>();
    if (unit >= 0) {
      const int g = lane >> 4, qq = (lane & 15) >> 2;
      const unsigned ro = SPARE + (4 * g + qq) * 320 + (lane & 3) * 8;
      const bool w1 = ufb < 8 * NS;
      const int f2 = ufb - 8 * NS;
      const unsigned wb = w1 ? WS_W1 + ((uct * 4 * NS + (ufb >> 1)) * 20 + (ufb & 1)) * 1024 : WS_W2 + ((uct * 2 * NT + (f2 >> 2)) * 20 + (f2 & 3) * 5) * 1024;
      const unsigned se = w1 ? 2048 : 1024, sh = w1 ? 10240 : NT * 20480;
      s16x8 tv[10];
#pragma unroll
      for (int e = 0; e < 10; ++e) {
        s16x4 lo = __builtin_amdgcn_ds_read_tr16_b64_v4i16((__attribute__((address_space(3))) s16x4*)(sm + ro + e * 32));
        s16x4 hi = __builtin_amdgcn_ds_read_tr16_b64_v4i16((__attribute__((address_space(3))) s16x4*)(sm + ro + e * 32 + 16 * 320));
        tv[e] = __builtin_shufflevector(lo, hi, 0, 1, 2, 3, 4, 5, 6, 7);
      }
#pragma unroll
      for (int e = 0; e < 10; ++e) st16(__builtin_bit_cast(u32x4, tv[e]), rws, lane * 16, wb + (e / 5) * sh + (e % 5) * se);
    }
    BARS(odd ? 14 - WO2 : 9 - WE2);
    waitv<0>();
    if (unit >= 0) PUBLISH(FL_WD + uct * 256 + ufb * 4);
    if (odd) {
      GATE(3, 1, fa, 64, PK_AB, {}, {});
    } else {
#pragma unroll
      for (int i = 0; i < QA; ++i) warmq(rq, vq, sq + i * 256);
      bar();
#pragma unroll
      for (int i = QA; i < QA + QB; ++i) warmq(rq, vq, sq + i * 256);
#pragma unroll 1
      for (int g = 0; g < 3; ++g) {
        glim = g < 2 ? GL0 << g : 64;
        GATE(g < 2 ? 1 : 4, 1, fa, 64, PK_AB, {}, {});
        if (g == 0) {
#pragma unroll 1
          for (int i = QA + QB; i < QN; ++i) warmq(rq, vq, sq + i * 256);
          STAMP(1);
        }
      }
    }
    BARS(odd ? 5 + HP : HP - 3);
    PUBLISH(FL_H + rt * 128 + (ct * 4 + kq) * 4);
    GATE(n3 - HP - 5, 2, f1o, 16, PK_A, {}, {});
    GATE(NS, 3, fh, 64, PK_H, {}, {});
    BARS(4);
    GATE(NT, 4, fs, 4, PK_SL, {}, {});
    STAMP(2);
    STAMP(3);
    if (bid == 0) {
#pragma unroll 1
      for (unsigned spin = 0; spin < SPIN_MAX; ++spin) {
        u32x4 v = __builtin_amdgcn_raw_buffer_load_b128(rws, FL_H + (lane >> 2) * 128 + (lane & 3) * 16, 0, 16);
        if (__all(v[0] == token && v[1] == token && v[2] == token && v[3] == token)) break;
        __builtin_amdgcn_s_sleep(1);
      }
      PUBLISH(WS_FLAG);
    }
    waitv<0>();
  }
}

extern "C" void kernel_launch(void* const* d_in, const int* in_sizes, int n_in, void* d_out, int out_size, void* d_ws, size_t ws_size,
                              hipStream_t stream) {
  (void)in_sizes; (void)n_in; (void)out_size; (void)ws_size;
  (void)hipFuncSetAttribute((const void*)fused_kernel, hipFuncAttributeMaxDynamicSharedMemorySize, LDS_BYTES);
  fused_kernel<<<256, 512, LDS_BYTES, stream>>>((const char*)d_in[0], (const char*)d_in[1], (const char*)d_in[2], (const char*)d_in[3],
                                                (const char*)d_in[4], (const char*)d_in[6], (const char*)d_in[7], (const char*)d_in[8],
                                                (const char*)d_in[9], (char*)d_out, (char*)d_ws);
}
```
